```python
import torch

def _softmax(x, axis=-1):
    return torch.softmax(x, dim=axis)
def _gelu(x):
    return torch.nn.functional.gelu(x, approximate="tanh")
def _silu(x):
    return torch.nn.functional.silu(x)
def _sigmoid(x):
    return torch.sigmoid(x)
def _layernorm(x, axis=-1, eps=1e-5):
    m = x.mean(dim=axis, keepdim=True)
    v = x.var(dim=axis, keepdim=True, unbiased=False)
    return (x - m) / torch.sqrt(v + eps)
def _scatter_add(tgt, idx, src, axis=0):
    return tgt.clone().index_add_(axis, idx, src)
def _segment_sum(src, seg, n):
    out = torch.zeros((n,) + src.shape[1:], dtype=src.dtype)
    return out.index_add_(0, seg, src)
def _pad(x, pw):
    flat = tuple(v for pair in reversed(pw) for v in pair)
    return torch.nn.functional.pad(x, flat)

kernel_name = 'gated_mix__sliding_block_cached__gqa_block_cached__decode'

def setup_inputs():

    _w = (lambda a: (lambda t: t.to(torch.bfloat16) if t.is_floating_point() else t)(torch.as_tensor(a)))
    def _A_setup():
        B, Sq, D = 2, 1, 576
        Hq, Hkv, Sk, Dh = 32, 16, 8192, 128
        assert Hq % Hkv == 0, (Hq, Hkv)
        return {
            "x": _w((lambda *s: torch.randn(*s, dtype=torch.bfloat16) * 0.05)(B, Sq, D)),
            "k_cache": _w((lambda *s: torch.randn(*s, dtype=torch.bfloat16) * 6.4)(B, Hkv, Sk, Dh)),
            "v_cache": _w((lambda *s: torch.randn(*s, dtype=torch.bfloat16) * 0.05)(B, Hkv, Sk, Dh)),
            "wq": _w((lambda *s: torch.randn(*s, dtype=torch.bfloat16) * 6.4)(D, Hq * Dh) * (D ** -0.5)),
            "wo": _w((lambda *s: torch.randn(*s, dtype=torch.bfloat16) * 0.05)(Hq * Dh, D) * ((Hq * Dh) ** -0.5)),
        }
    _A_in = _A_setup()

    def _B_setup():
        B, Sq, D = 2, 1, 576
        Hq, Hkv, Sk, Dh = 32, 16, 8192, 128
        assert Hq % Hkv == 0, (Hq, Hkv)
        return {
            "x": _w((lambda *s: torch.randn(*s, dtype=torch.bfloat16) * 0.05)(B, Sq, D)),
            "k_cache": _w((lambda *s: torch.randn(*s, dtype=torch.bfloat16) * 6.4)(B, Hkv, Sk, Dh)),
            "v_cache": _w((lambda *s: torch.randn(*s, dtype=torch.bfloat16) * 0.05)(B, Hkv, Sk, Dh)),
            "wq": _w((lambda *s: torch.randn(*s, dtype=torch.bfloat16) * 6.4)(D, Hq * Dh) * (D ** -0.5)),
            "wo": _w((lambda *s: torch.randn(*s, dtype=torch.bfloat16) * 0.05)(Hq * Dh, D) * ((Hq * Dh) ** -0.5)),
        }
    _B_in = _B_setup()

    return {
        "A_x": _A_in["x"],
        "A_k_cache": _A_in["k_cache"],
        "A_v_cache": _A_in["v_cache"],
        "A_wq": _A_in["wq"],
        "A_wo": _A_in["wo"],
        "B_x": _B_in["x"],
        "B_k_cache": _B_in["k_cache"],
        "B_v_cache": _B_in["v_cache"],
        "B_wq": _B_in["wq"],
        "B_wo": _B_in["wo"],
        "gate_w": _w((lambda *s: torch.randn(*s, dtype=torch.bfloat16) * 0.05)(576) * 16.0),
    }

def reference(A_x, A_k_cache, A_v_cache, A_wq, A_wo, B_x, B_k_cache, B_v_cache, B_wq, B_wo, gate_w):
    def _A_ref(x, k_cache, v_cache, wq, wo):
        B, Sq, D = x.shape
        Hq, Dh = 32, 128
        Hkv, Sk = k_cache.shape[1], k_cache.shape[2]
        W = min(4096, max(256, Sk // 4))
        rep = Hq // Hkv
        q = (lambda x, *d: x.permute(*d))((x @ wq).reshape(B, Sq, Hq, Dh), 0, 2, 1, 3)
        k = (lambda x, n, axis: torch.repeat_interleave(x, n, dim=axis))(k_cache[:, :, Sk - W:, :], rep, axis=1)
        v = (lambda x, n, axis: torch.repeat_interleave(x, n, dim=axis))(v_cache[:, :, Sk - W:, :], rep, axis=1)
        scores = torch.einsum("bhqd,bhkd->bhqk", q, k) * (Dh ** -0.5)
        p = _softmax(scores, axis=-1)
        o = torch.einsum("bhqk,bhkd->bhqd", p, v)
        return (lambda x, *d: x.permute(*d))(o, 0, 2, 1, 3).reshape(B, Sq, Hq * Dh) @ wo

    def _B_ref(x, k_cache, v_cache, wq, wo):
        B, Sq, D = x.shape
        Hq, Dh = 32, 128
        Hkv = k_cache.shape[1]
        rep = Hq // Hkv
        q = (lambda x, *d: x.permute(*d))((x @ wq).reshape(B, Sq, Hq, Dh), 0, 2, 1, 3)
        k = (lambda x, n, axis: torch.repeat_interleave(x, n, dim=axis))(k_cache, rep, axis=1)
        v = (lambda x, n, axis: torch.repeat_interleave(x, n, dim=axis))(v_cache, rep, axis=1)
        scores = torch.einsum("bhqd,bhkd->bhqk", q, k) * (Dh ** -0.5)
        p = _softmax(scores, axis=-1)
        o = torch.einsum("bhqk,bhkd->bhqd", p, v)
        return (lambda x, *d: x.permute(*d))(o, 0, 2, 1, 3).reshape(B, Sq, Hq * Dh) @ wo

    x = A_x
    g = _sigmoid((x * gate_w).sum(-1, keepdims=True))
    a = _A_ref(x, A_k_cache, A_v_cache, A_wq, A_wo)
    b = _B_ref(x, B_k_cache, B_v_cache, B_wq, B_wo)
    return g * a + (1 - g) * b
```

```cpp
#include <hip/hip_runtime.h>
#include <hip/hip_bf16.h>
#include <stdint.h>

typedef unsigned u32;
typedef unsigned v4u __attribute__((ext_vector_type(4)));
typedef unsigned v2u __attribute__((ext_vector_type(2)));
typedef float f32x4 __attribute__((ext_vector_type(4)));
typedef short bf16x8 __attribute__((ext_vector_type(8)));
typedef __bf16 bf16x2 __attribute__((ext_vector_type(2)));
#define GAS __attribute__((address_space(1)))

constexpr int DM = 576, HQ = 32, HKV = 16, SK = 8192, DH = 128, WA = 2048, NB = 2;
constexpr int NQ = HQ * DH;
constexpr float C2 = 0.088388347648318440f * 1.4426950408889634f;

__device__ __forceinline__ float dot2(u32 k, u32 q, float acc) {
  return __builtin_amdgcn_fdot2_f32_bf16(__builtin_bit_cast(bf16x2, k), __builtin_bit_cast(bf16x2, q), acc, false);
}
__device__ __forceinline__ u32 cvtpk(float lo, float hi) {
  bf16x2 r; r[0] = (__bf16)lo; r[1] = (__bf16)hi; return __builtin_bit_cast(u32, r);
}
template <int CTRL> __device__ __forceinline__ float dppf(float x) {
  return __builtin_bit_cast(float, __builtin_amdgcn_mov_dpp(__builtin_bit_cast(int, x), CTRL, 0xf, 0xf, true));
}
template <int CTRL> __device__ __forceinline__ u32 dppu(u32 x) {
  return (u32)__builtin_amdgcn_mov_dpp((int)x, CTRL, 0xf, 0xf, true);
}
__device__ __forceinline__ float bflo(u32 x) { return __builtin_bit_cast(float, x << 16); }
__device__ __forceinline__ float bfhi(u32 x) { return __builtin_bit_cast(float, x & 0xffff0000u); }
__device__ __forceinline__ float bf2f(unsigned short b) { return __builtin_bit_cast(float, (u32)b << 16); }
__device__ __forceinline__ f32x4 mfma(v4u a, v4u b, f32x4 c) {
  return __builtin_amdgcn_mfma_f32_16x16x32_bf16(__builtin_bit_cast(bf16x8, a), __builtin_bit_cast(bf16x8, b), c, 0, 0, 0);
}
__device__ __forceinline__ float xrow_max(float x) {
  auto s = __builtin_amdgcn_permlane16_swap(__float_as_uint(x), __float_as_uint(x), false, false);
  x = fmaxf(__uint_as_float(s[0]), __uint_as_float(s[1]));
  auto t = __builtin_amdgcn_permlane32_swap(__float_as_uint(x), __float_as_uint(x), false, false);
  return fmaxf(__uint_as_float(t[0]), __uint_as_float(t[1]));
}
__device__ __forceinline__ float xrow_sum(float x) {
  auto s = __builtin_amdgcn_permlane16_swap(__float_as_uint(x), __float_as_uint(x), false, false);
  x = __uint_as_float(s[0]) + __uint_as_float(s[1]);
  auto t = __builtin_amdgcn_permlane32_swap(__float_as_uint(x), __float_as_uint(x), false, false);
  return __uint_as_float(t[0]) + __uint_as_float(t[1]);
}
constexpr int XOR1 = 0xB1, XOR2 = 0x4E, ROR4 = 0x124, ROR8 = 0x128, ROR12 = 0x12C;

constexpr size_t WS_Q = 0;
constexpr size_t WS_P = 65536;
constexpr size_t WS_Y = WS_P + 2560 * 2 * 132 * 4;

__global__ void __launch_bounds__(256) k_q(const unsigned short* x, const unsigned short* wqA, const unsigned short* wqB, unsigned short* q) {
  __shared__ float red[4][64];
  const int w = blockIdx.x, br = w >> 7, col0 = (w & 127) * 32;
  const int tid = threadIdx.x, lane = tid & 63, wave = tid >> 6;
  const unsigned short* wq = br ? wqB : wqA;
  const int rr = tid >> 2, cc = tid & 3;
  float acc[2][8];
  for (int b = 0; b < 2; ++b) for (int j = 0; j < 8; ++j) acc[b][j] = 0.f;
  v4u wv[9]; float x0[9], x1[9];
#pragma unroll
  for (int i = 0; i < 9; ++i) {
    const int row = rr + 64 * i;
    wv[i] = __builtin_nontemporal_load((const v4u*)(wq + (size_t)row * NQ + col0 + 8 * cc));
    x0[i] = bf2f(x[row]); x1[i] = bf2f(x[DM + row]);
  }
#pragma unroll
  for (int i = 0; i < 9; ++i) {
#pragma unroll
    for (int k = 0; k < 4; ++k) {
      const float lo = bflo(wv[i][k]), hi = bfhi(wv[i][k]);
      acc[0][2 * k] = fmaf(x0[i], lo, acc[0][2 * k]); acc[0][2 * k + 1] = fmaf(x0[i], hi, acc[0][2 * k + 1]);
      acc[1][2 * k] = fmaf(x1[i], lo, acc[1][2 * k]); acc[1][2 * k + 1] = fmaf(x1[i], hi, acc[1][2 * k + 1]);
    }
  }
#pragma unroll
  for (int b = 0; b < 2; ++b)
#pragma unroll
    for (int j = 0; j < 8; ++j) {
      float v = acc[b][j];
      v += dppf<ROR4>(v); v += dppf<ROR8>(v); v = xrow_sum(v);
      if (lane < 4) red[wave][b * 32 + lane * 8 + j] = v;
    }
  __syncthreads();
  if (tid < 64) {
    const float s = (red[0][tid] + red[1][tid]) + (red[2][tid] + red[3][tid]);
    const int b = tid >> 5, c = tid & 31;
    q[(br * 2 + b) * NQ + col0 + c] = (unsigned short)(cvtpk(s, s) & 0xffff);
  }
}

constexpr int WIN = 24;
__global__ void __launch_bounds__(640, 3) k_attn(const unsigned short* kA, const unsigned short* vA, const unsigned short* kB, const unsigned short* vB,
                                                 const unsigned short* q, float* part) {
  const int tid = threadIdx.x, lane = tid & 63, wave = __builtin_amdgcn_readfirstlane(tid >> 6);
  const int wg = blockIdx.x, g = 2 * (wg & 7) + (wg >> 7), j = (wg >> 3) & 15;
  const int u = 10 * j + wave;
  int br, b, key0;
  if (u < 32) { br = 0; b = u >> 4; key0 = (SK - WA) + (u & 15) * 128; }
  else { br = 1; b = (u - 32) >> 6; key0 = ((u - 32) & 63) * 128; }
  const size_t off = ((size_t)(b * HKV + g) * SK + key0) * DH;
  const GAS v4u* kp = (const GAS v4u*)((br ? kB : kA) + off) + lane;
  const GAS v4u* vp = (const GAS v4u*)((br ? vB : vA) + off) + lane;
  const int c = lane & 15;
  const int hown = (c >> 1) & 1;
  const unsigned short* qb = q + (br * 2 + b) * NQ + (2 * g) * DH + 8 * c;
  const v4u qa = *(const v4u*)(qb + hown * DH), qo = *(const v4u*)(qb + (hown ^ 1) * DH);
  v4u ring[WIN];
#pragma unroll
  for (int i = 0; i < WIN; ++i) ring[i] = __builtin_nontemporal_load(i < 32 ? kp + i * 64 : vp + (i - 32) * 64);
  __builtin_amdgcn_sched_barrier(0);
  float sc[8];
#pragma unroll
  for (int t = 0; t < 8; ++t) {
    float y[4];
#pragma unroll
    for (int n = 0; n < 4; ++n) {
      const int i = 4 * t + n;
      const v4u kk = ring[i % WIN];
      float xa = 0.f, xo = 0.f;
#pragma unroll
      for (int k = 0; k < 4; ++k) { xa = dot2(kk[k], qa[k], xa); xo = dot2(kk[k], qo[k], xo); }
      y[n] = xa + dppf<XOR2>(xo);
      const int nx = i + WIN;
      ring[i % WIN] = __builtin_nontemporal_load(nx < 32 ? kp + nx * 64 : vp + (nx - 32) * 64);
    }
    const float a0 = y[0] + dppf<ROR8>(y[0]), a1 = y[1] + dppf<ROR8>(y[1]);
    const float a2 = y[2] + dppf<ROR8>(y[2]), a3 = y[3] + dppf<ROR8>(y[3]);
    const float b0 = (c & 8) ? a2 : a0, b1 = (c & 8) ? a3 : a1;
    const float e0 = b0 + dppf<0x104>(b0);
    const float e1 = b1 + dppf<0x114>(b1);
    float s = (c & 4) ? e1 : e0;
    s += dppf<XOR1>(s);
    sc[t] = s;
    __builtin_amdgcn_sched_barrier(0);
  }
  float m = fmaxf(fmaxf(fmaxf(sc[0], sc[1]), fmaxf(sc[2], sc[3])), fmaxf(fmaxf(sc[4], sc[5]), fmaxf(sc[6], sc[7])));
  m = fmaxf(m, dppf<ROR4>(m)); m = fmaxf(m, dppf<ROR8>(m)); m = xrow_max(m);
  const float mc = m * C2;
  float l = 0.f; u32 pp[8];
#pragma unroll
  for (int t = 0; t < 8; ++t) { const float p = __builtin_amdgcn_exp2f(fmaf(sc[t], C2, -mc)); l += p; pp[t] = cvtpk(p, p); }
  l += dppf<ROR4>(l); l += dppf<ROR8>(l); l = xrow_sum(l);
  const u32 half = (c & 1) ? 0xffff0000u : 0x0000ffffu;
  u32 msk[4];
#pragma unroll
  for (int k = 0; k < 4; ++k) msk[k] = ((c >> 2) == k) ? half : 0u;
  f32x4 acc = {0.f, 0.f, 0.f, 0.f};
#pragma unroll
  for (int t = 0; t < 8; ++t) {
    const u32 r0 = pp[t], r1 = dppu<ROR4>(r0), r2 = dppu<ROR8>(r0), r3 = dppu<ROR12>(r0);
#pragma unroll
    for (int n = 0; n < 4; ++n) {
      const int i = 32 + 4 * t + n;
      v4u a;
#pragma unroll
      for (int k = 0; k < 4; ++k) { const int d = (k - n) & 3; a[k] = (d == 0 ? r0 : d == 1 ? r1 : d == 2 ? r2 : r3) & msk[k]; }
      acc = mfma(a, ring[i % WIN], acc);
      const int nx = i + WIN;
      if (nx < 64) ring[i % WIN] = __builtin_nontemporal_load(vp + (nx - 32) * 64);
    }
    __builtin_amdgcn_sched_barrier(0);
  }
  float* po = part + (size_t)(g * 160 + u) * 2 * 132;
  const int d0 = 8 * c + 2 * (lane >> 4);
  *(float2*)(po + 4 + d0) = make_float2(acc[0], acc[1]);
  *(float2*)(po + 132 + 4 + d0) = make_float2(acc[2], acc[3]);
  if (lane == 0 || lane == 2) { float* pm = po + (lane >> 1) * 132; pm[0] = mc; pm[1] = l; }
}

__global__ void __launch_bounds__(576) k_comb(const float* part, const unsigned short* woA, const unsigned short* woB, float* y) {
  __shared__ float o[2][32];
  __shared__ float red[8][2][576];
  const int wg = blockIdx.x, g = wg >> 4, j = wg & 15, br = j >> 3, h = (j >> 2) & 1, dq = j & 3;
  const int tid = threadIdx.x;
  const unsigned short* wo = (br ? woB : woA) + (size_t)((2 * g + h) * DH + 32 * dq) * DM;
  const int ch = tid % 72, rg = tid / 72;
  v4u wv[4];
#pragma unroll
  for (int i = 0; i < 4; ++i) wv[i] = __builtin_nontemporal_load((const v4u*)(wo + (size_t)(4 * rg + i) * DM + 8 * ch));
  if (tid < 512) {
    const int b = tid >> 8, d = tid & 31, sub = (tid >> 5) & 7;
    const int nu = br ? 64 : 16, u0 = br ? 32 + 64 * b : 16 * b;
    const float* p0 = part + ((size_t)(g * 160 + u0) * 2 + h) * 132;
    float M = -INFINITY;
    for (int u = 0; u < nu; ++u) M = fmaxf(M, p0[(size_t)u * 264]);
    float L = 0.f, a = 0.f;
    for (int u = 0; u < nu; ++u) { const float w = __builtin_amdgcn_exp2f(p0[(size_t)u * 264] - M); L = fmaf(w, p0[(size_t)u * 264 + 1], L);
      if ((u & 7) == sub) a = fmaf(w, p0[(size_t)u * 264 + 4 + 32 * dq + d], a); }
    red[sub][b][d] = a / L;
  }
  __syncthreads();
  if (tid < 64) { float s = 0.f; for (int k = 0; k < 8; ++k) s += red[k][tid >> 5][tid & 31]; o[tid >> 5][tid & 31] = s; }
  __syncthreads();
  float acc[2][8];
  for (int b = 0; b < 2; ++b) for (int k = 0; k < 8; ++k) acc[b][k] = 0.f;
#pragma unroll
  for (int i = 0; i < 4; ++i) {
    const float o0 = o[0][4 * rg + i], o1 = o[1][4 * rg + i];
#pragma unroll
    for (int k = 0; k < 4; ++k) {
      const float lo = bflo(wv[i][k]), hi = bfhi(wv[i][k]);
      acc[0][2 * k] = fmaf(o0, lo, acc[0][2 * k]); acc[0][2 * k + 1] = fmaf(o0, hi, acc[0][2 * k + 1]);
      acc[1][2 * k] = fmaf(o1, lo, acc[1][2 * k]); acc[1][2 * k + 1] = fmaf(o1, hi, acc[1][2 * k + 1]);
    }
  }
  for (int b = 0; b < 2; ++b) for (int k = 0; k < 8; ++k) red[rg][b][8 * ch + k] = acc[b][k];
  __syncthreads();
  for (int b = 0; b < 2; ++b) { float s = 0.f; for (int k = 0; k < 8; ++k) s += red[k][b][tid]; y[((size_t)wg * 2 + b) * DM + tid] = s; }
}

__global__ void __launch_bounds__(1024) k_fin(const float* y, const unsigned short* x, const unsigned short* gw, unsigned short* out) {
  __shared__ float red[2][16][64];
  __shared__ float gs[2];
  const int tid = threadIdx.x, o = blockIdx.x * 64 + (tid & 63), g = tid >> 6;
  float sa = 0.f, sb = 0.f;
#pragma unroll
  for (int j = 0; j < 8; ++j) { sa += y[(size_t)(g * 16 + j) * 1152 + o]; sb += y[(size_t)(g * 16 + 8 + j) * 1152 + o]; }
  red[0][g][tid & 63] = sa; red[1][g][tid & 63] = sb;
  if (tid < 128) {
    const int b = tid >> 6, l = tid & 63; float s = 0.f;
    for (int i = l; i < DM; i += 64) s = fmaf(bf2f(x[b * DM + i]), bf2f(gw[i]), s);
    for (int k = 32; k; k >>= 1) s += __shfl_xor(s, k);
    if (l == 0) gs[b] = 1.f / (1.f + __expf(-s));
  }
  __syncthreads();
  if (tid < 64) {
    float a = 0.f, bb = 0.f;
    for (int k = 0; k < 16; ++k) { a += red[0][k][tid]; bb += red[1][k][tid]; }
    const float gg = gs[o / DM];
    const float r = gg * a + (1.f - gg) * bb;
    out[o] = (unsigned short)(cvtpk(r, r) & 0xffff);
  }
}

extern "C" void kernel_launch(void* const* d_in, const int* in_sizes, int n_in, void* d_out, int out_size, void* d_ws, size_t ws_size, hipStream_t stream) {
  const unsigned short* x = (const unsigned short*)d_in[0];
  const unsigned short *kA = (const unsigned short*)d_in[1], *vA = (const unsigned short*)d_in[2], *wqA = (const unsigned short*)d_in[3], *woA = (const unsigned short*)d_in[4];
  const unsigned short *kB = (const unsigned short*)d_in[6], *vB = (const unsigned short*)d_in[7], *wqB = (const unsigned short*)d_in[8], *woB = (const unsigned short*)d_in[9];
  const unsigned short* gw = (const unsigned short*)d_in[10];
  char* ws = (char*)d_ws;
  k_q<<<256, 256, 0, stream>>>(x, wqA, wqB, (unsigned short*)(ws + WS_Q));
  k_attn<<<256, 640, 0, stream>>>(kA, vA, kB, vB, (const unsigned short*)(ws + WS_Q), (float*)(ws + WS_P));
  k_comb<<<256, 576, 0, stream>>>((const float*)(ws + WS_P), woA, woB, (float*)(ws + WS_Y));
  k_fin<<<18, 1024, 0, stream>>>((const float*)(ws + WS_Y), x, gw, (unsigned short*)d_out);
}
```

```cpp
#define AUX 2
#include <hip/hip_runtime.h>
#include <hip/hip_bf16.h>
#include <stdint.h>

typedef unsigned u32;
typedef unsigned v4u __attribute__((ext_vector_type(4)));
typedef unsigned v2u __attribute__((ext_vector_type(2)));
typedef float f32x4 __attribute__((ext_vector_type(4)));
typedef short bf16x8 __attribute__((ext_vector_type(8)));
typedef __bf16 bf16x2 __attribute__((ext_vector_type(2)));
#define GAS __attribute__((address_space(1)))

constexpr int DM = 576, HQ = 32, HKV = 16, SK = 8192, DH = 128, WA = 2048, NB = 2;
constexpr int NQ = HQ * DH;
constexpr float C2 = 0.088388347648318440f * 1.4426950408889634f;

__device__ __forceinline__ float dot2(u32 k, u32 q, float acc) {
  return __builtin_amdgcn_fdot2_f32_bf16(__builtin_bit_cast(bf16x2, k), __builtin_bit_cast(bf16x2, q), acc, false);
}
__device__ __forceinline__ u32 cvtpk(float lo, float hi) {
  bf16x2 r; r[0] = (__bf16)lo; r[1] = (__bf16)hi; return __builtin_bit_cast(u32, r);
}
template <int CTRL> __device__ __forceinline__ float dppf(float x) {
  return __builtin_bit_cast(float, __builtin_amdgcn_mov_dpp(__builtin_bit_cast(int, x), CTRL, 0xf, 0xf, true));
}
template <int CTRL> __device__ __forceinline__ u32 dppu(u32 x) {
  return (u32)__builtin_amdgcn_mov_dpp((int)x, CTRL, 0xf, 0xf, true);
}
__device__ __forceinline__ float bflo(u32 x) { return __builtin_bit_cast(float, x << 16); }
__device__ __forceinline__ float bfhi(u32 x) { return __builtin_bit_cast(float, x & 0xffff0000u); }
__device__ __forceinline__ float bf2f(unsigned short b) { return __builtin_bit_cast(float, (u32)b << 16); }
__device__ __forceinline__ f32x4 mfma(v4u a, v4u b, f32x4 c) {
  return __builtin_amdgcn_mfma_f32_16x16x32_bf16(__builtin_bit_cast(bf16x8, a), __builtin_bit_cast(bf16x8, b), c, 0, 0, 0);
}
__device__ __forceinline__ float xrow_max(float x) {
  auto s = __builtin_amdgcn_permlane16_swap(__float_as_uint(x), __float_as_uint(x), false, false);
  x = fmaxf(__uint_as_float(s[0]), __uint_as_float(s[1]));
  auto t = __builtin_amdgcn_permlane32_swap(__float_as_uint(x), __float_as_uint(x), false, false);
  return fmaxf(__uint_as_float(t[0]), __uint_as_float(t[1]));
}
__device__ __forceinline__ float xrow_sum(float x) {
  auto s = __builtin_amdgcn_permlane16_swap(__float_as_uint(x), __float_as_uint(x), false, false);
  x = __uint_as_float(s[0]) + __uint_as_float(s[1]);
  auto t = __builtin_amdgcn_permlane32_swap(__float_as_uint(x), __float_as_uint(x), false, false);
  return __uint_as_float(t[0]) + __uint_as_float(t[1]);
}
constexpr int XOR1 = 0xB1, XOR2 = 0x4E, ROR4 = 0x124, ROR8 = 0x128, ROR12 = 0x12C;

constexpr size_t WS_Q = 0;
constexpr size_t WS_P = 65536;
constexpr size_t WS_Y = WS_P + 2560 * 2 * 132 * 4;

__global__ void __launch_bounds__(256) k_q(const unsigned short* x, const unsigned short* wqA, const unsigned short* wqB, unsigned short* q) {
  __shared__ float red[4][64];
  const int w = blockIdx.x, br = w >> 7, col0 = (w & 127) * 32;
  const int tid = threadIdx.x, lane = tid & 63, wave = tid >> 6;
  const unsigned short* wq = br ? wqB : wqA;
  const int rr = tid >> 2, cc = tid & 3;
  float acc[2][8];
  for (int b = 0; b < 2; ++b) for (int j = 0; j < 8; ++j) acc[b][j] = 0.f;
  v4u wv[9]; float x0[9], x1[9];
#pragma unroll
  for (int i = 0; i < 9; ++i) {
    const int row = rr + 64 * i;
    wv[i] = __builtin_nontemporal_load((const v4u*)(wq + (size_t)row * NQ + col0 + 8 * cc));
    x0[i] = bf2f(x[row]); x1[i] = bf2f(x[DM + row]);
  }
#pragma unroll
  for (int i = 0; i < 9; ++i) {
#pragma unroll
    for (int k = 0; k < 4; ++k) {
      const float lo = bflo(wv[i][k]), hi = bfhi(wv[i][k]);
      acc[0][2 * k] = fmaf(x0[i], lo, acc[0][2 * k]); acc[0][2 * k + 1] = fmaf(x0[i], hi, acc[0][2 * k + 1]);
      acc[1][2 * k] = fmaf(x1[i], lo, acc[1][2 * k]); acc[1][2 * k + 1] = fmaf(x1[i], hi, acc[1][2 * k + 1]);
    }
  }
#pragma unroll
  for (int b = 0; b < 2; ++b)
#pragma unroll
    for (int j = 0; j < 8; ++j) {
      float v = acc[b][j];
      v += dppf<ROR4>(v); v += dppf<ROR8>(v); v = xrow_sum(v);
      if (lane < 4) red[wave][b * 32 + lane * 8 + j] = v;
    }
  __syncthreads();
  if (tid < 64) {
    const float s = (red[0][tid] + red[1][tid]) + (red[2][tid] + red[3][tid]);
    const int b = tid >> 5, c = tid & 31;
    q[(br * 2 + b) * NQ + col0 + c] = (unsigned short)(cvtpk(s, s) & 0xffff);
  }
}

constexpr int WIN = 24;
__global__ void __launch_bounds__(640, 3) k_attn(const unsigned short* kA, const unsigned short* vA, const unsigned short* kB, const unsigned short* vB,
                                                 const unsigned short* q, float* part) {
  const int tid = threadIdx.x, lane = tid & 63, wave = __builtin_amdgcn_readfirstlane(tid >> 6);
  const int wg = blockIdx.x, g = 2 * (wg & 7) + (wg >> 7), j = (wg >> 3) & 15;
  const int u = 10 * j + wave;
  int br, b, key0;
  if (u < 32) { br = 0; b = u >> 4; key0 = (SK - WA) + (u & 15) * 128; }
  else { br = 1; b = (u - 32) >> 6; key0 = ((u - 32) & 63) * 128; }
  const size_t off = ((size_t)(b * HKV + g) * SK + key0) * DH;
  const __amdgpu_buffer_rsrc_t rk = __builtin_amdgcn_make_buffer_rsrc((void*)((br ? kB : kA) + off), 0, 128 * 256, 0x00020000);
  const __amdgpu_buffer_rsrc_t rv = __builtin_amdgcn_make_buffer_rsrc((void*)((br ? vB : vA) + off), 0, 128 * 256, 0x00020000);
  const int vo = lane * 16;
#define LD(i) __builtin_amdgcn_raw_buffer_load_b128((i) < 32 ? rk : rv, vo, ((i) & 31) * 1024, AUX)
  const int c = lane & 15;
  const int hown = (c >> 1) & 1;
  const unsigned short* qb = q + (br * 2 + b) * NQ + (2 * g) * DH + 8 * c;
  const v4u qa = *(const v4u*)(qb + hown * DH), qo = *(const v4u*)(qb + (hown ^ 1) * DH);
  v4u ring[WIN];
#pragma unroll
  for (int i = 0; i < WIN; ++i) { ring[i] = LD(i); __builtin_amdgcn_sched_barrier(0); }
  float sc[8];
#pragma unroll
  for (int t = 0; t < 8; ++t) {
    float y[4];
#pragma unroll
    for (int n = 0; n < 4; ++n) {
      const int i = 4 * t + n;
      const v4u kk = ring[i % WIN];
      float xa = 0.f, xo = 0.f;
#pragma unroll
      for (int k = 0; k < 4; ++k) { xa = dot2(kk[k], qa[k], xa); xo = dot2(kk[k], qo[k], xo); }
      y[n] = xa + dppf<XOR2>(xo);
      const int nx = i + WIN;
      __builtin_amdgcn_sched_barrier(0); ring[i % WIN] = LD(nx); __builtin_amdgcn_sched_barrier(0);
    }
    const float a0 = y[0] + dppf<ROR8>(y[0]), a1 = y[1] + dppf<ROR8>(y[1]);
    const float a2 = y[2] + dppf<ROR8>(y[2]), a3 = y[3] + dppf<ROR8>(y[3]);
    const float b0 = (c & 8) ? a2 : a0, b1 = (c & 8) ? a3 : a1;
    const float e0 = b0 + dppf<0x104>(b0);
    const float e1 = b1 + dppf<0x114>(b1);
    float s = (c & 4) ? e1 : e0;
    s += dppf<XOR1>(s);
    sc[t] = s;
    __builtin_amdgcn_sched_barrier(0);
  }
  float m = fmaxf(fmaxf(fmaxf(sc[0], sc[1]), fmaxf(sc[2], sc[3])), fmaxf(fmaxf(sc[4], sc[5]), fmaxf(sc[6], sc[7])));
  m = fmaxf(m, dppf<ROR4>(m)); m = fmaxf(m, dppf<ROR8>(m)); m = xrow_max(m);
  const float mc = m * C2;
  float l = 0.f; u32 pp[8];
#pragma unroll
  for (int t = 0; t < 8; ++t) { const float p = __builtin_amdgcn_exp2f(fmaf(sc[t], C2, -mc)); l += p; pp[t] = cvtpk(p, p); }
  l += dppf<ROR4>(l); l += dppf<ROR8>(l); l = xrow_sum(l);
  const u32 half = (c & 1) ? 0xffff0000u : 0x0000ffffu;
  u32 msk[4];
#pragma unroll
  for (int k = 0; k < 4; ++k) msk[k] = ((c >> 2) == k) ? half : 0u;
  f32x4 acc = {0.f, 0.f, 0.f, 0.f};
#pragma unroll
  for (int t = 0; t < 8; ++t) {
    const u32 r0 = pp[t], r1 = dppu<ROR4>(r0), r2 = dppu<ROR8>(r0), r3 = dppu<ROR12>(r0);
#pragma unroll
    for (int n = 0; n < 4; ++n) {
      const int i = 32 + 4 * t + n;
      v4u a;
#pragma unroll
      for (int k = 0; k < 4; ++k) { const int d = (k - n) & 3; a[k] = (d == 0 ? r0 : d == 1 ? r1 : d == 2 ? r2 : r3) & msk[k]; }
      acc = mfma(a, ring[i % WIN], acc);
      const int nx = i + WIN;
      __builtin_amdgcn_sched_barrier(0); if (nx < 64) ring[i % WIN] = LD(nx); __builtin_amdgcn_sched_barrier(0);
    }
    __builtin_amdgcn_sched_barrier(0);
  }
  float* po = part + (size_t)(g * 160 + u) * 2 * 132;
  const int d0 = 8 * c + 2 * (lane >> 4);
  *(float2*)(po + 4 + d0) = make_float2(acc[0], acc[1]);
  *(float2*)(po + 132 + 4 + d0) = make_float2(acc[2], acc[3]);
  if (lane == 0 || lane == 2) { float* pm = po + (lane >> 1) * 132; pm[0] = mc; pm[1] = l; }
}

__global__ void __launch_bounds__(576) k_comb(const float* part, const unsigned short* woA, const unsigned short* woB, float* y) {
  __shared__ float o[2][32];
  __shared__ float red[8][2][576];
  const int wg = blockIdx.x, g = wg >> 4, j = wg & 15, br = j >> 3, h = (j >> 2) & 1, dq = j & 3;
  const int tid = threadIdx.x;
  const unsigned short* wo = (br ? woB : woA) + (size_t)((2 * g + h) * DH + 32 * dq) * DM;
  const int ch = tid % 72, rg = tid / 72;
  v4u wv[4];
#pragma unroll
  for (int i = 0; i < 4; ++i) wv[i] = __builtin_nontemporal_load((const v4u*)(wo + (size_t)(4 * rg + i) * DM + 8 * ch));
  if (tid < 512) {
    const int b = tid >> 8, d = tid & 31, sub = (tid >> 5) & 7;
    const int nu = br ? 64 : 16, u0 = br ? 32 + 64 * b : 16 * b;
    const float* p0 = part + ((size_t)(g * 160 + u0) * 2 + h) * 132;
    float M = -INFINITY;
    for (int u = 0; u < nu; ++u) M = fmaxf(M, p0[(size_t)u * 264]);
    float L = 0.f, a = 0.f;
    for (int u = 0; u < nu; ++u) { const float w = __builtin_amdgcn_exp2f(p0[(size_t)u * 264] - M); L = fmaf(w, p0[(size_t)u * 264 + 1], L);
      if ((u & 7) == sub) a = fmaf(w, p0[(size_t)u * 264 + 4 + 32 * dq + d], a); }
    red[sub][b][d] = a / L;
  }
  __syncthreads();
  if (tid < 64) { float s = 0.f; for (int k = 0; k < 8; ++k) s += red[k][tid >> 5][tid & 31]; o[tid >> 5][tid & 31] = s; }
  __syncthreads();
  float acc[2][8];
  for (int b = 0; b < 2; ++b) for (int k = 0; k < 8; ++k) acc[b][k] = 0.f;
#pragma unroll
  for (int i = 0; i < 4; ++i) {
    const float o0 = o[0][4 * rg + i], o1 = o[1][4 * rg + i];
#pragma unroll
    for (int k = 0; k < 4; ++k) {
      const float lo = bflo(wv[i][k]), hi = bfhi(wv[i][k]);
      acc[0][2 * k] = fmaf(o0, lo, acc[0][2 * k]); acc[0][2 * k + 1] = fmaf(o0, hi, acc[0][2 * k + 1]);
      acc[1][2 * k] = fmaf(o1, lo, acc[1][2 * k]); acc[1][2 * k + 1] = fmaf(o1, hi, acc[1][2 * k + 1]);
    }
  }
  for (int b = 0; b < 2; ++b) for (int k = 0; k < 8; ++k) red[rg][b][8 * ch + k] = acc[b][k];
  __syncthreads();
  for (int b = 0; b < 2; ++b) { float s = 0.f; for (int k = 0; k < 8; ++k) s += red[k][b][tid]; y[((size_t)wg * 2 + b) * DM + tid] = s; }
}

__global__ void __launch_bounds__(1024) k_fin(const float* y, const unsigned short* x, const unsigned short* gw, unsigned short* out) {
  __shared__ float red[2][16][64];
  __shared__ float gs[2];
  const int tid = threadIdx.x, o = blockIdx.x * 64 + (tid & 63), g = tid >> 6;
  float sa = 0.f, sb = 0.f;
#pragma unroll
  for (int j = 0; j < 8; ++j) { sa += y[(size_t)(g * 16 + j) * 1152 + o]; sb += y[(size_t)(g * 16 + 8 + j) * 1152 + o]; }
  red[0][g][tid & 63] = sa; red[1][g][tid & 63] = sb;
  if (tid < 128) {
    const int b = tid >> 6, l = tid & 63; float s = 0.f;
    for (int i = l; i < DM; i += 64) s = fmaf(bf2f(x[b * DM + i]), bf2f(gw[i]), s);
    for (int k = 32; k; k >>= 1) s += __shfl_xor(s, k);
    if (l == 0) gs[b] = 1.f / (1.f + __expf(-s));
  }
  __syncthreads();
  if (tid < 64) {
    float a = 0.f, bb = 0.f;
    for (int k = 0; k < 16; ++k) { a += red[0][k][tid]; bb += red[1][k][tid]; }
    const float gg = gs[o / DM];
    const float r = gg * a + (1.f - gg) * bb;
    out[o] = (unsigned short)(cvtpk(r, r) & 0xffff);
  }
}

extern "C" void kernel_launch(void* const* d_in, const int* in_sizes, int n_in, void* d_out, int out_size, void* d_ws, size_t ws_size, hipStream_t stream) {
  const unsigned short* x = (const unsigned short*)d_in[0];
  const unsigned short *kA = (const unsigned short*)d_in[1], *vA = (const unsigned short*)d_in[2], *wqA = (const unsigned short*)d_in[3], *woA = (const unsigned short*)d_in[4];
  const unsigned short *kB = (const unsigned short*)d_in[6], *vB = (const unsigned short*)d_in[7], *wqB = (const unsigned short*)d_in[8], *woB = (const unsigned short*)d_in[9];
  const unsigned short* gw = (const unsigned short*)d_in[10];
  char* ws = (char*)d_ws;
  k_q<<<256, 256, 0, stream>>>(x, wqA, wqB, (unsigned short*)(ws + WS_Q));
  k_attn<<<256, 640, 0, stream>>>(kA, vA, kB, vB, (const unsigned short*)(ws + WS_Q), (float*)(ws + WS_P));
  k_comb<<<256, 576, 0, stream>>>((const float*)(ws + WS_P), woA, woB, (float*)(ws + WS_Y));
  k_fin<<<18, 1024, 0, stream>>>((const float*)(ws + WS_Y), x, gw, (unsigned short*)d_out);
}
```

```cpp
#include <hip/hip_runtime.h>
#include <hip/hip_bf16.h>
#include <stdint.h>

typedef unsigned u32;
typedef unsigned short u16;
typedef unsigned v4u __attribute__((ext_vector_type(4)));
typedef unsigned v2u __attribute__((ext_vector_type(2)));
typedef float f32x4 __attribute__((ext_vector_type(4)));
typedef float f32x2 __attribute__((ext_vector_type(2)));
typedef short v4s __attribute__((ext_vector_type(4)));
typedef short bf16x8 __attribute__((ext_vector_type(8)));
typedef __bf16 bf16x2 __attribute__((ext_vector_type(2)));
#define LAS __attribute__((address_space(3)))
#define SB() __builtin_amdgcn_sched_barrier(0)
typedef __amdgpu_buffer_rsrc_t rsrc_t;

constexpr int DM = 576, HKV = 16, SK = 8192, DH = 128, WA = 2048, NQ = 4096;
constexpr float SCALE = 0.088388347648318440f, LOG2E = 1.4426950408889634f;
constexpr int AUX_NT = 2, AUX_SC1 = 16;
#define PRE 24
#define FL 4
#define GG 4
constexpr u32 SPIN_CAP = 1u << 18;

constexpr int WS_GEN = 0;
constexpr int WS_Q = 4096;
constexpr int WS_ML = WS_Q + 16 * 4 * 1024;
constexpr int WS_ACC = WS_ML + 16 * 4 * 256;
constexpr int WS_Y = WS_ACC + 16 * 2 * 10 * 2 * 1024;
constexpr int WS_END = WS_Y + 144 * 4096;
#define STAMP(k)

constexpr int WOSTR = 304;
constexpr int L_WO = 0;
constexpr int L_PART = L_WO + 128 * WOSTR;
constexpr int L_PML = L_PART + 2 * 8192;
constexpr int L_QP = L_PML + 2 * 128;
constexpr int L_QS = L_QP + 9 * 256;
constexpr int L_OS = L_QS + 1024;
constexpr int L_G = L_OS + 512;
constexpr int L_END = L_G + 16;

__device__ __forceinline__ float dot2(u32 k, u32 q, float acc) {
  return __builtin_amdgcn_fdot2_f32_bf16(__builtin_bit_cast(bf16x2, k), __builtin_bit_cast(bf16x2, q), acc, false);
}
__device__ __forceinline__ u32 cvtpk(float lo, float hi) {
  bf16x2 r; r[0] = (__bf16)lo; r[1] = (__bf16)hi; return __builtin_bit_cast(u32, r);
}
__device__ __forceinline__ float rbf(float x) { return __builtin_bit_cast(float, cvtpk(x, x) & 0xffff0000u); }
template <int CTRL> __device__ __forceinline__ float dppf(float x) {
  return __builtin_bit_cast(float, __builtin_amdgcn_mov_dpp(__builtin_bit_cast(int, x), CTRL, 0xf, 0xf, true));
}
template <int CTRL> __device__ __forceinline__ u32 dppu(u32 x) { return (u32)__builtin_amdgcn_mov_dpp((int)x, CTRL, 0xf, 0xf, true); }
__device__ __forceinline__ float asf(u32 x) { return __builtin_bit_cast(float, x); }
__device__ __forceinline__ u32 asu(float x) { return __builtin_bit_cast(u32, x); }
__device__ __forceinline__ f32x4 mfma(v4u a, v4u b, f32x4 c) {
  return __builtin_amdgcn_mfma_f32_16x16x32_bf16(__builtin_bit_cast(bf16x8, a), __builtin_bit_cast(bf16x8, b), c, 0, 0, 0);
}
__device__ __forceinline__ float xrow_max(float x) {
  auto s = __builtin_amdgcn_permlane16_swap(asu(x), asu(x), false, false);
  x = fmaxf(asf(s[0]), asf(s[1]));
  auto t = __builtin_amdgcn_permlane32_swap(asu(x), asu(x), false, false);
  return fmaxf(asf(t[0]), asf(t[1]));
}
__device__ __forceinline__ float xrow_sum(float x) {
  auto s = __builtin_amdgcn_permlane16_swap(asu(x), asu(x), false, false);
  x = asf(s[0]) + asf(s[1]);
  auto t = __builtin_amdgcn_permlane32_swap(asu(x), asu(x), false, false);
  return asf(t[0]) + asf(t[1]);
}
constexpr int XOR1 = 0xB1, XOR2 = 0x4E, ROR4 = 0x124, ROR8 = 0x128, ROR12 = 0x12C, SHL4 = 0x104, SHR4 = 0x114;
__device__ __forceinline__ rsrc_t mkrs(const void* p, int bytes) { return __builtin_amdgcn_make_buffer_rsrc((void*)p, 0, bytes, 0x00020000); }
template <int N> __device__ __forceinline__ void wait_vm() { __builtin_amdgcn_s_waitcnt(0x0F70 | (N & 15) | ((N >> 4) << 14)); }
__device__ __forceinline__ void wg_barrier() {
  __builtin_amdgcn_fence(__ATOMIC_RELEASE, "workgroup", "local");
  __builtin_amdgcn_s_barrier();
  __builtin_amdgcn_fence(__ATOMIC_ACQUIRE, "workgroup", "local");
}
__device__ __forceinline__ void put_piece(rsrc_t rs, int off, u32 a, u32 b, u32 tag) {
  __builtin_amdgcn_raw_buffer_store_b128(v4u{a, tag, b, tag}, rs, off, 0, AUX_SC1);
}
template <int N, int SLEEP> __device__ __forceinline__ void get_pieces(rsrc_t rs, const int (&off)[N], u32 tag, v4u (&v)[N]) {
  bool ok = false;
#pragma nounroll
  for (u32 spin = 0; spin < SPIN_CAP; ++spin) {
    if (!ok) {
#pragma unroll
      for (int k = 0; k < N; ++k) v[k] = __builtin_amdgcn_raw_buffer_load_b128(rs, off[k], 0, AUX_SC1);
      ok = true;
#pragma unroll
      for (int k = 0; k < N; ++k) ok &= (v[k][1] == tag) & (v[k][3] == tag);
    }
    if (__builtin_amdgcn_ballot_w64(!ok) == 0) break;
    if (SLEEP) __builtin_amdgcn_s_sleep(SLEEP);
    asm volatile("" ::: "memory");
  }
}

__device__ __forceinline__ float qk_part(v4u kk, v4u qa, v4u qo) {
  float xa = 0.f, xo = 0.f;
#pragma unroll
  for (int k = 0; k < 4; ++k) { xa = dot2(kk[k], qa[k], xa); xo = dot2(kk[k], qo[k], xo); }
  return xa + dppf<XOR2>(xo);
}
__device__ __forceinline__ float qk_red(const float (&y)[4], int c) {
  const float a0 = y[0] + dppf<ROR8>(y[0]), a1 = y[1] + dppf<ROR8>(y[1]);
  const float a2 = y[2] + dppf<ROR8>(y[2]), a3 = y[3] + dppf<ROR8>(y[3]);
  const float b0 = (c & 8) ? a2 : a0, b1 = (c & 8) ? a3 : a1;
  const float e0 = b0 + dppf<SHL4>(b0), e1 = b1 + dppf<SHR4>(b1);
  float s = (c & 4) ? e1 : e0;
  s += dppf<XOR1>(s);
  return rbf(rbf(s) * SCALE) * LOG2E;
}
template <int N> __device__ __forceinline__ f32x4 pv_step(u32 pp, const u32 (&msk)[4], v4u v, f32x4 acc) {
  v4u a;
  a[(N + 0) & 3] = pp & msk[(N + 0) & 3];
  a[(N + 1) & 3] = dppu<ROR4>(pp) & msk[(N + 1) & 3];
  a[(N + 2) & 3] = dppu<ROR8>(pp) & msk[(N + 2) & 3];
  a[(N + 3) & 3] = dppu<ROR12>(pp) & msk[(N + 3) & 3];
  return mfma(a, v, acc);
}

__device__ __forceinline__ void merge_publish(LAS char* L, int buf, int h, int lane, rsrc_t rw, int team, int slot, u32 tag) {
  const LAS f32x4* ml = (const LAS f32x4*)(L + L_PML + buf * 128 + h * 64);
  const LAS f32x2* pa = (const LAS f32x2*)(L + L_PART + buf * 8192 + h * 512) + lane;
  f32x4 q[4]; f32x2 a[8];
#pragma unroll
  for (int i = 0; i < 4; ++i) q[i] = ml[i];
#pragma unroll
  for (int w = 0; w < 8; ++w) a[w] = pa[w * 128];
  const float M = fmaxf(fmaxf(fmaxf(q[0][0], q[0][2]), fmaxf(q[1][0], q[1][2])), fmaxf(fmaxf(q[2][0], q[2][2]), fmaxf(q[3][0], q[3][2])));
  float Ls = 0.f, o0 = 0.f, o1 = 0.f;
#pragma unroll
  for (int w = 0; w < 8; ++w) {
    const float wt = __builtin_amdgcn_exp2f(q[w >> 1][(w & 1) * 2] - M);
    Ls = fmaf(wt, q[w >> 1][(w & 1) * 2 + 1], Ls); o0 = fmaf(wt, a[w][0], o0); o1 = fmaf(wt, a[w][1], o1);
  }
  put_piece(rw, WS_ACC + ((team * 10 + slot) * 2 + h) * 1024 + lane * 16, asu(o0), asu(o1), tag);
  if (lane == 0) put_piece(rw, WS_ML + (team * 2 + h) * 256 + slot * 16, asu(M), asu(Ls), tag);
}

__global__ void __launch_bounds__(576, 3)
k_fused(const u16* __restrict__ x, const u16* __restrict__ kA, const u16* __restrict__ vA, const u16* __restrict__ wqA, const u16* __restrict__ woA,
        const u16* __restrict__ kB, const u16* __restrict__ vB, const u16* __restrict__ wqB, const u16* __restrict__ woB,
        const u16* __restrict__ gw, u16* __restrict__ out, char* ws) {
  __shared__ __attribute__((aligned(16))) char lds[L_END];
  LAS char* L = (LAS char*)lds;
  const int tid = threadIdx.x, lane = tid & 63, wave = __builtin_amdgcn_readfirstlane(tid >> 6);
  const int wg = blockIdx.x, m = wg & 7, team = wg >> 3, g = team >> 1, b = team & 1;
  const int j = 2 * m + b;
  const bool isA = m >= 6;
  const int qbr = m < 4 ? 1 : 0;
  const int th = m >> 2, tdh = (m >> 1) & 1, tcb = j & 3;
  STAMP(0);
  const rsrc_t rw = mkrs(ws, WS_END);
  const u32 tag = *(const u32*)(ws + WS_GEN) + 1u;

  const rsrc_t rq = mkrs((qbr ? wqB : wqA) + g * 256 + (j & 7) * 32, (DM * NQ - g * 256 - (j & 7) * 32) * 2);
  const int qrow = wave * 64 + (lane >> 2) * 4;
  v4u wq[4];
#pragma unroll
  for (int i = 0; i < 4; ++i) { wq[i] = __builtin_amdgcn_raw_buffer_load_b128(rq, qrow * (NQ * 2) + (lane & 3) * 16, i * (NQ * 2), AUX_NT); SB(); }
  const v2u x0 = *(const v2u*)(x + qrow), x1 = *(const v2u*)(x + DM + qrow);
  SB();
  const int woc = tid % 18, wor = tid / 18;
  const int wodim = 8 * ((wor >> 1) & 15) + 4 * tdh + (wor & 1);
  const int wooff = (((2 * g + th) * DH + wodim) * DM + tcb * 144 + woc * 8) * 2;
  const rsrc_t roA = mkrs(woA, NQ * DM * 2), roB = mkrs(woB, NQ * DM * 2);
  v4u wo[4];
#pragma unroll
  for (int i = 0; i < 4; ++i) { wait_vm<5>(); SB(); wo[i] = __builtin_amdgcn_raw_buffer_load_b128(i < 2 ? roA : roB, wooff, (i & 1) * 2 * DM * 2, AUX_NT); SB(); }
  {
    float s[16];
#pragma unroll
    for (int k = 0; k < 4; ++k) {
      const u32 l01 = __builtin_amdgcn_perm(wq[1][k], wq[0][k], 0x05040100u), h01 = __builtin_amdgcn_perm(wq[1][k], wq[0][k], 0x07060302u);
      const u32 l23 = __builtin_amdgcn_perm(wq[3][k], wq[2][k], 0x05040100u), h23 = __builtin_amdgcn_perm(wq[3][k], wq[2][k], 0x07060302u);
      s[2 * k] = dot2(l23, x0[1], dot2(l01, x0[0], 0.f)); s[2 * k + 1] = dot2(h23, x0[1], dot2(h01, x0[0], 0.f));
      s[8 + 2 * k] = dot2(l23, x1[1], dot2(l01, x1[0], 0.f)); s[8 + 2 * k + 1] = dot2(h23, x1[1], dot2(h01, x1[0], 0.f));
    }
#pragma unroll
    for (int i = 0; i < 8; ++i) { auto t = __builtin_amdgcn_permlane32_swap(asu(s[i]), asu(s[i + 8]), false, false); s[i] = asf(t[0]) + asf(t[1]); }
#pragma unroll
    for (int i = 0; i < 4; ++i) { auto t = __builtin_amdgcn_permlane16_swap(asu(s[i]), asu(s[i + 4]), false, false); s[i] = asf(t[0]) + asf(t[1]); }
    const float a0 = s[0] + dppf<ROR8>(s[0]), a1 = s[1] + dppf<ROR8>(s[1]), a2 = s[2] + dppf<ROR8>(s[2]), a3 = s[3] + dppf<ROR8>(s[3]);
    const float b0 = (lane & 8) ? a2 : a0, b1 = (lane & 8) ? a3 : a1;
    const float e0 = b0 + dppf<SHL4>(b0), e1 = b1 + dppf<SHR4>(b1);
    const float r = (lane & 4) ? e1 : e0;
    ((LAS float*)(L + L_QP))[wave * 64 + (lane >> 5) * 32 + (lane & 3) * 8 + ((lane >> 2) & 7)] = r;
  }
  wg_barrier();
  STAMP(1);
  LAS char* wodst = L + L_WO + wor * WOSTR + woc * 16;

  if (wave == 8) {
    __builtin_amdgcn_s_setprio(3);
    {
      float s = 0.f;
#pragma unroll
      for (int w = 0; w < 9; ++w) s += ((LAS float*)(L + L_QP))[w * 64 + lane];
      const u32 pk = cvtpk(s, dppf<XOR1>(s));
      const u32 pk2 = dppu<XOR2>(pk);
      if ((lane & 3) == 0) put_piece(rw, WS_Q + ((g * 2 + qbr) * 2 + (lane >> 5)) * 1024 + ((j & 7) * 8 + ((lane & 31) >> 2)) * 16, pk, pk2, tag);
    }
    STAMP(2);
#pragma unroll
    for (int i = 0; i < 4; ++i) *(LAS v4u*)(wodst + i * 32 * WOSTR) = wo[i];
    {
      const int off[2] = {WS_Q + ((g * 2 + 1) * 2 + b) * 1024 + lane * 16, WS_Q + ((g * 2 + (isA ? 0 : 1)) * 2 + b) * 1024 + lane * 16};
      v4u v[2];
      get_pieces<2, 2>(rw, off, tag, v);
      *(LAS v2u*)(L + L_QS + 512 + lane * 8) = v2u{v[0][0], v[0][2]};
      *(LAS v2u*)(L + L_QS + lane * 8) = v2u{v[1][0], v[1][2]};
    }
    STAMP(3);
    wg_barrier();
    {
      const int gb = lane >> 5, gl = lane & 31;
      float z = 0.f;
#pragma unroll
      for (int i = 0; i < 9; ++i) {
        const u32 xv = *(const u32*)(x + gb * DM + 2 * gl + 64 * i), gv = *(const u32*)(gw + 2 * gl + 64 * i);
        z += rbf(asf(xv << 16) * asf(gv << 16)) + rbf(asf(xv & 0xffff0000u) * asf(gv & 0xffff0000u));
      }
      z += dppf<XOR1>(z); z += dppf<XOR2>(z); z += dppf<ROR4>(z); z += dppf<ROR8>(z);
      auto t = __builtin_amdgcn_permlane16_swap(asu(z), asu(z), false, false);
      z = rbf(asf(t[0]) + asf(t[1]));
      const float gt = rbf(1.f / (1.f + __expf(-z)));
      if (gl == 0) *(LAS f32x2*)(L + L_G + gb * 8) = f32x2{gt, rbf(1.f - gt)};
    }
    if (isA) {
      wg_barrier();
      merge_publish(L, 0, 0, lane, rw, team, 8 + (m - 6), tag);
      merge_publish(L, 0, 1, lane, rw, team, 8 + (m - 6), tag);
    }
    {
      const int tb = 2 * g + (lane >> 5);
      const int oa = WS_ACC + ((tb * 10 + 8) * 2 + th) * 1024 + (32 * tdh + (lane & 31)) * 16, om = WS_ML + (tb * 2 + th) * 256 + 8 * 16;
      const int off[4] = {oa, oa + 2048, om, om + 16};
      v4u v[4];
      get_pieces<4, 32>(rw, off, tag, v);
      const float m0 = asf(v[2][0]), m1 = asf(v[3][0]), M = fmaxf(m0, m1);
      const float w0 = __builtin_amdgcn_exp2f(m0 - M), w1 = __builtin_amdgcn_exp2f(m1 - M);
      const float inv = 1.f / (w0 * asf(v[2][2]) + w1 * asf(v[3][2]));
      ((LAS u32*)(L + L_OS))[lane] = cvtpk((w0 * asf(v[0][0]) + w1 * asf(v[1][0])) * inv, (w0 * asf(v[0][2]) + w1 * asf(v[1][2])) * inv);
    }
    STAMP(4);
    wg_barrier();
  } else {
    const size_t seg = (size_t)(b * HKV + g) * SK * DH;
    const int key1 = isA ? (SK - WA) + (m - 6) * 1024 : m * 1024;
    const rsrc_t rk1 = mkrs((isA ? kA : kB) + seg + (size_t)key1 * DH, 1024 * 256), rv1 = mkrs((isA ? vA : vB) + seg + (size_t)key1 * DH, 1024 * 256);
    const rsrc_t rk2 = mkrs(kB + seg + (size_t)(6144 + 256 * m) * DH, 256 * 256), rv2 = mkrs(vB + seg + (size_t)(6144 + 256 * m) * DH, 256 * 256);
    const int vo = lane * 16 + wave * GG * 1024;
    const int c = lane & 15;
    constexpr int NT = 80;
    v4u ld[NT];
#define LIDX(i) ((i) < 64 ? ((i) & 31) : ((i) & 7))
#define ISSUE(i) do { if ((i) < NT) { ld[(i) < NT ? (i) : 0] = __builtin_amdgcn_raw_buffer_load_b128((i) < 32 ? rk1 : (i) < 64 ? rv1 : (i) < 72 ? rk2 : rv2, vo, \
                        ((LIDX(i) / GG) * 8 * GG + LIDX(i) % GG) * 1024, AUX_NT); SB(); } } while (0)
#pragma unroll
    for (int i = 0; i < PRE; ++i) {
      wait_vm<FL - 1>(); SB();
      ISSUE(i);
      if (i < 4) { *(LAS v4u*)(wodst + i * 32 * WOSTR) = wo[i]; SB(); }
    }
    STAMP(2);
    wg_barrier();
    STAMP(3);
    const int hown = (c >> 1) & 1;
    v4u qa = *(const LAS v4u*)(L + L_QS + hown * 256 + c * 16), qo = *(const LAS v4u*)(L + L_QS + (hown ^ 1) * 256 + c * 16);
    float sc[8];
    {
      int dummy = 0; (void)dummy;
#define KTILE(t, I0, I1) do { float y[4]; \
        _Pragma("unroll") for (int n = 0; n < 4; ++n) { y[n] = qk_part(ld[4 * (t) + n], qa, qo); SB(); \
          if ((I0) + n < (I1)) { wait_vm<FL - 1>(); SB(); ISSUE((I0) + n); } } \
        sc[t] = qk_red(y, c); SB(); } while (0)
#define IP(t) ((t) < 0 ? PRE : ((PRE + (t) + 1) > (4 * (t) + 4 + FL) ? (PRE + (t) + 1) : (4 * (t) + 4 + FL)))
      KTILE(0, IP(-1), IP(0)); KTILE(1, IP(0), IP(1)); KTILE(2, IP(1), IP(2)); KTILE(3, IP(2), IP(3));
      KTILE(4, IP(3), IP(4)); KTILE(5, IP(4), IP(5)); KTILE(6, IP(5), IP(6)); KTILE(7, IP(6), IP(7));
    }
    static_assert(IP(7) == 32 + FL && IP(7) - IP(6) <= 4 && IP(6) - IP(5) <= 4, "schedule");
    float mx = fmaxf(fmaxf(fmaxf(sc[0], sc[1]), fmaxf(sc[2], sc[3])), fmaxf(fmaxf(sc[4], sc[5]), fmaxf(sc[6], sc[7])));
    mx = fmaxf(mx, dppf<ROR4>(mx)); mx = fmaxf(mx, dppf<ROR8>(mx)); mx = xrow_max(mx);
    float l = 0.f; u32 pp[8];
#pragma unroll
    for (int t = 0; t < 8; ++t) { const float p = __builtin_amdgcn_exp2f(sc[t] - mx); l += p; pp[t] = cvtpk(p, p); }
    const u32 half = (c & 1) ? 0xffff0000u : 0x0000ffffu;
    u32 msk[4];
#pragma unroll
    for (int k = 0; k < 4; ++k) msk[k] = ((c >> 2) == k) ? half : 0u;
    f32x4 acc = {0.f, 0.f, 0.f, 0.f};
    SB();
    STAMP(4);
#define VTILE(t, base, P) do { acc = pv_step<0>(P, msk, ld[(base) + 4 * (t) + 0], acc); SB(); ISSUE((base) + 4 * (t) + 0 + FL); \
      acc = pv_step<1>(P, msk, ld[(base) + 4 * (t) + 1], acc); SB(); ISSUE((base) + 4 * (t) + 1 + FL); \
      acc = pv_step<2>(P, msk, ld[(base) + 4 * (t) + 2], acc); SB(); ISSUE((base) + 4 * (t) + 2 + FL); \
      acc = pv_step<3>(P, msk, ld[(base) + 4 * (t) + 3], acc); SB(); ISSUE((base) + 4 * (t) + 3 + FL); } while (0)
    VTILE(0, 32, pp[0]); VTILE(1, 32, pp[1]); VTILE(2, 32, pp[2]); VTILE(3, 32, pp[3]);
    VTILE(4, 32, pp[4]); VTILE(5, 32, pp[5]); VTILE(6, 32, pp[6]); VTILE(7, 32, pp[7]);
    STAMP(5);
    if (isA) {
      float lt = l; lt += dppf<ROR4>(lt); lt += dppf<ROR8>(lt); lt = xrow_sum(lt);
      *(LAS f32x2*)(L + L_PART + wave * 1024 + lane * 8) = f32x2{acc[0], acc[1]};
      *(LAS f32x2*)(L + L_PART + wave * 1024 + 512 + lane * 8) = f32x2{acc[2], acc[3]};
      if ((lane & ~2) == 0) *(LAS f32x2*)(L + L_PML + (lane >> 1) * 64 + wave * 8) = f32x2{mx, lt};
      wg_barrier();
      mx = -INFINITY; l = 0.f; acc = f32x4{0.f, 0.f, 0.f, 0.f};
      qa = *(const LAS v4u*)(L + L_QS + 512 + hown * 256 + c * 16); qo = *(const LAS v4u*)(L + L_QS + 512 + (hown ^ 1) * 256 + c * 16);
    }
    float s2[2];
#pragma unroll
    for (int t = 0; t < 2; ++t) {
      float y[4];
#pragma unroll
      for (int n = 0; n < 4; ++n) { y[n] = qk_part(ld[64 + 4 * t + n], qa, qo); SB(); ISSUE(64 + 4 * t + n + FL); }
      s2[t] = qk_red(y, c); SB();
    }
    float m2 = fmaxf(s2[0], s2[1]);
    m2 = fmaxf(m2, dppf<ROR4>(m2)); m2 = fmaxf(m2, dppf<ROR8>(m2)); m2 = fmaxf(xrow_max(m2), mx);
    const float al = __builtin_amdgcn_exp2f(mx - m2);
    const float p0 = __builtin_amdgcn_exp2f(s2[0] - m2), p1 = __builtin_amdgcn_exp2f(s2[1] - m2);
    l = fmaf(l, al, p0 + p1);
    const float al0 = dppf<0x00>(al), al1 = dppf<0xAA>(al);
    acc[0] *= al0; acc[1] *= al0; acc[2] *= al1; acc[3] *= al1;
    const u32 pq0 = cvtpk(p0, p0), pq1 = cvtpk(p1, p1);
    l += dppf<ROR4>(l); l += dppf<ROR8>(l); l = xrow_sum(l);
    if ((lane & ~2) == 0) *(LAS f32x2*)(L + L_PML + 128 + (lane >> 1) * 64 + wave * 8) = f32x2{m2, l};
    SB();
    VTILE(0, 72, pq0); VTILE(1, 72, pq1);
    *(LAS f32x2*)(L + L_PART + 8192 + wave * 1024 + lane * 8) = f32x2{acc[0], acc[1]};
    *(LAS f32x2*)(L + L_PART + 8192 + wave * 1024 + 512 + lane * 8) = f32x2{acc[2], acc[3]};
    STAMP(6);
    wg_barrier();
    if (wave < 2) merge_publish(L, 1, wave, lane, rw, team, m, tag);
    STAMP(7);
    {
      const int tb = 2 * g + (wave >> 2), s = lane >> 3;
      const int off[2] = {WS_ACC + ((tb * 10 + s) * 2 + th) * 1024 + (32 * tdh + 8 * (wave & 3) + (lane & 7)) * 16, WS_ML + (tb * 2 + th) * 256 + s * 16};
      v4u v[2];
      get_pieces<2, 0>(rw, off, tag, v);
      STAMP(8);
      const float ms = asf(v[1][0]);
      const float M = xrow_max(fmaxf(ms, dppf<ROR8>(ms)));
      const float wt = __builtin_amdgcn_exp2f(ms - M);
      float Ls = wt * asf(v[1][2]), o0 = wt * asf(v[0][0]), o1 = wt * asf(v[0][2]);
      Ls += dppf<ROR8>(Ls); o0 += dppf<ROR8>(o0); o1 += dppf<ROR8>(o1);
      Ls = xrow_sum(Ls); o0 = xrow_sum(o0); o1 = xrow_sum(o1);
      const float inv = 1.f / Ls;
      if (lane < 8) ((LAS u32*)(L + L_OS + 256))[wave * 8 + lane] = cvtpk(o0 * inv, o1 * inv);
    }
  }
  v4u bf[4];
  {
    const LAS char* p = L + L_WO + (8 * (lane >> 4) + ((lane >> 2) & 3)) * WOSTR + (wave * 16 + (lane & 3) * 4) * 2;
#pragma unroll
    for (int s = 0; s < 4; ++s) {
      const v2u lo = __builtin_bit_cast(v2u, __builtin_amdgcn_ds_read_tr16_b64_v4i16((LAS v4s*)(p + s * 32 * WOSTR)));
      const v2u hi = __builtin_bit_cast(v2u, __builtin_amdgcn_ds_read_tr16_b64_v4i16((LAS v4s*)(p + (s * 32 + 4) * WOSTR)));
      bf[s] = v4u{lo[0], lo[1], hi[0], hi[1]};
    }
  }
  wg_barrier();
  STAMP(9);
  {
    const LAS char* p = L + L_OS + (lane & 1) * 128 + (lane >> 4) * 16;
    f32x4 da = {0.f, 0.f, 0.f, 0.f}, db = {0.f, 0.f, 0.f, 0.f};
    da = mfma(*(const LAS v4u*)(p), bf[0], da); da = mfma(*(const LAS v4u*)(p + 64), bf[1], da);
    db = mfma(*(const LAS v4u*)(p + 256), bf[2], db); db = mfma(*(const LAS v4u*)(p + 320), bf[3], db);
    const f32x4 gt = *(const LAS f32x4*)(L + L_G);
    const float y0 = gt[0] * da[0] + gt[1] * db[0], y1 = gt[2] * da[1] + gt[3] * db[1];
    const int col = tcb * 144 + wave * 16 + lane;
    if (lane < 16) put_piece(rw, WS_Y + (col >> 2) * 4096 + (4 * g + (m >> 1)) * 64 + (col & 3) * 16, asu(y0), asu(y1), tag);
  }
  STAMP(10);
  if (wave == 8 && wg < 144) {
    const int o = WS_Y + wg * 4096 + lane * 16;
    const int off[4] = {o, o + 1024, o + 2048, o + 3072};
    v4u v[4];
    get_pieces<4, 0>(rw, off, tag, v);
    STAMP(11);
    float y0 = (asf(v[0][0]) + asf(v[1][0])) + (asf(v[2][0]) + asf(v[3][0])), y1 = (asf(v[0][2]) + asf(v[1][2])) + (asf(v[2][2]) + asf(v[3][2]));
    y0 += dppf<ROR4>(y0); y1 += dppf<ROR4>(y1); y0 += dppf<ROR8>(y0); y1 += dppf<ROR8>(y1);
    y0 = xrow_sum(y0); y1 = xrow_sum(y1);
    const float r = (lane & 4) ? y1 : y0;
    if (lane < 8) out[(lane >> 2) * DM + 4 * wg + (lane & 3)] = (u16)(cvtpk(r, r) & 0xffffu);
    if (wg == 0 && lane == 0) *(u32*)(ws + WS_GEN) = tag;
    STAMP(12);
  }
}

extern "C" void kernel_launch(void* const* d_in, const int* in_sizes, int n_in, void* d_out, int out_size, void* d_ws, size_t ws_size, hipStream_t stream) {
  k_fused<<<256, 576, 0, stream>>>((const u16*)d_in[0], (const u16*)d_in[1], (const u16*)d_in[2], (const u16*)d_in[3], (const u16*)d_in[4],
                                   (const u16*)d_in[6], (const u16*)d_in[7], (const u16*)d_in[8], (const u16*)d_in[9], (const u16*)d_in[10],
                                   (u16*)d_out, (char*)d_ws);
}
```

```cpp
#define DIAG 100
#define DIAGH 0
#define DIAGK 0
#define DIAGMIN 0
#include <hip/hip_runtime.h>
#include <hip/hip_bf16.h>
#include <stdint.h>

typedef unsigned u32;
typedef unsigned short u16;
typedef unsigned v4u __attribute__((ext_vector_type(4)));
typedef unsigned v2u __attribute__((ext_vector_type(2)));
typedef float f32x4 __attribute__((ext_vector_type(4)));
typedef float f32x2 __attribute__((ext_vector_type(2)));
typedef short v4s __attribute__((ext_vector_type(4)));
typedef short bf16x8 __attribute__((ext_vector_type(8)));
typedef __bf16 bf16x2 __attribute__((ext_vector_type(2)));
#define LAS __attribute__((address_space(3)))
#define SB() __builtin_amdgcn_sched_barrier(0)
typedef __amdgpu_buffer_rsrc_t rsrc_t;

constexpr int DM = 576, HKV = 16, SK = 8192, DH = 128, WA = 2048, NQ = 4096;
constexpr float SCALE = 0.088388347648318440f, LOG2E = 1.4426950408889634f;
constexpr int AUX_NT = 2, AUX_SC1 = 16;
#define PRE 24
#define FL 4
#define GG 4
constexpr u32 SPIN_CAP = 1u << 18;

constexpr int WS_GEN = 0;
constexpr int WS_Q = 4096;
constexpr int WS_ML = WS_Q + 16 * 4 * 1024;
constexpr int WS_ACC = WS_ML + 16 * 4 * 256;
constexpr int WS_Y = WS_ACC + 16 * 2 * 10 * 2 * 1024;
constexpr int WS_END = WS_Y + 144 * 4096;
constexpr int WS_DG = 2 << 20;
#define STAMP(k) do { if (((k) == 0 || (k) == DIAG) && lane == 0) { const u32 t_ = (u32)__builtin_amdgcn_s_memrealtime(); const u32 tg_ = *(const u32*)(ws + WS_GEN) + 1u; \
    put_piece(mkrs(ws + WS_DG, 1 << 20), ((wg * 9 + wave) * 2 + ((k) ? 1 : 0)) * 16, t_, t_, tg_); } } while (0)

constexpr int WOSTR = 304;
constexpr int L_WO = 0;
constexpr int L_PART = L_WO + 128 * WOSTR;
constexpr int L_PML = L_PART + 2 * 8192;
constexpr int L_QP = L_PML + 2 * 128;
constexpr int L_QS = L_QP + 9 * 256;
constexpr int L_OS = L_QS + 1024;
constexpr int L_G = L_OS + 512;
constexpr int L_END = L_G + 16;

__device__ __forceinline__ float dot2(u32 k, u32 q, float acc) {
  return __builtin_amdgcn_fdot2_f32_bf16(__builtin_bit_cast(bf16x2, k), __builtin_bit_cast(bf16x2, q), acc, false);
}
__device__ __forceinline__ u32 cvtpk(float lo, float hi) {
  bf16x2 r; r[0] = (__bf16)lo; r[1] = (__bf16)hi; return __builtin_bit_cast(u32, r);
}
__device__ __forceinline__ float rbf(float x) { return __builtin_bit_cast(float, cvtpk(x, x) & 0xffff0000u); }
template <int CTRL> __device__ __forceinline__ float dppf(float x) {
  return __builtin_bit_cast(float, __builtin_amdgcn_mov_dpp(__builtin_bit_cast(int, x), CTRL, 0xf, 0xf, true));
}
template <int CTRL> __device__ __forceinline__ u32 dppu(u32 x) { return (u32)__builtin_amdgcn_mov_dpp((int)x, CTRL, 0xf, 0xf, true); }
__device__ __forceinline__ float asf(u32 x) { return __builtin_bit_cast(float, x); }
__device__ __forceinline__ u32 asu(float x) { return __builtin_bit_cast(u32, x); }
__device__ __forceinline__ f32x4 mfma(v4u a, v4u b, f32x4 c) {
  return __builtin_amdgcn_mfma_f32_16x16x32_bf16(__builtin_bit_cast(bf16x8, a), __builtin_bit_cast(bf16x8, b), c, 0, 0, 0);
}
__device__ __forceinline__ float xrow_max(float x) {
  auto s = __builtin_amdgcn_permlane16_swap(asu(x), asu(x), false, false);
  x = fmaxf(asf(s[0]), asf(s[1]));
  auto t = __builtin_amdgcn_permlane32_swap(asu(x), asu(x), false, false);
  return fmaxf(asf(t[0]), asf(t[1]));
}
__device__ __forceinline__ float xrow_sum(float x) {
  auto s = __builtin_amdgcn_permlane16_swap(asu(x), asu(x), false, false);
  x = asf(s[0]) + asf(s[1]);
  auto t = __builtin_amdgcn_permlane32_swap(asu(x), asu(x), false, false);
  return asf(t[0]) + asf(t[1]);
}
constexpr int XOR1 = 0xB1, XOR2 = 0x4E, ROR4 = 0x124, ROR8 = 0x128, ROR12 = 0x12C, SHL4 = 0x104, SHR4 = 0x114;
__device__ __forceinline__ rsrc_t mkrs(const void* p, int bytes) { return __builtin_amdgcn_make_buffer_rsrc((void*)p, 0, bytes, 0x00020000); }
template <int N> __device__ __forceinline__ void wait_vm() { __builtin_amdgcn_s_waitcnt(0x0F70 | (N & 15) | ((N >> 4) << 14)); }
__device__ __forceinline__ void wg_barrier() {
  __builtin_amdgcn_fence(__ATOMIC_RELEASE, "workgroup", "local");
  __builtin_amdgcn_s_barrier();
  __builtin_amdgcn_fence(__ATOMIC_ACQUIRE, "workgroup", "local");
}
__device__ __forceinline__ void put_piece(rsrc_t rs, int off, u32 a, u32 b, u32 tag) {
  __builtin_amdgcn_raw_buffer_store_b128(v4u{a, tag, b, tag}, rs, off, 0, AUX_SC1);
}
template <int N, int SLEEP> __device__ __forceinline__ void get_pieces(rsrc_t rs, const int (&off)[N], u32 tag, v4u (&v)[N]) {
  bool ok = false;
#pragma nounroll
  for (u32 spin = 0; spin < SPIN_CAP; ++spin) {
    if (!ok) {
#pragma unroll
      for (int k = 0; k < N; ++k) v[k] = __builtin_amdgcn_raw_buffer_load_b128(rs, off[k], 0, AUX_SC1);
      ok = true;
#pragma unroll
      for (int k = 0; k < N; ++k) ok &= (v[k][1] == tag) & (v[k][3] == tag);
    }
    if (__builtin_amdgcn_ballot_w64(!ok) == 0) break;
    if (SLEEP) __builtin_amdgcn_s_sleep(SLEEP);
    asm volatile("" ::: "memory");
  }
}

__device__ __forceinline__ float qk_part(v4u kk, v4u qa, v4u qo) {
  float xa = 0.f, xo = 0.f;
#pragma unroll
  for (int k = 0; k < 4; ++k) { xa = dot2(kk[k], qa[k], xa); xo = dot2(kk[k], qo[k], xo); }
  return xa + dppf<XOR2>(xo);
}
__device__ __forceinline__ float qk_red(const float (&y)[4], int c) {
  const float a0 = y[0] + dppf<ROR8>(y[0]), a1 = y[1] + dppf<ROR8>(y[1]);
  const float a2 = y[2] + dppf<ROR8>(y[2]), a3 = y[3] + dppf<ROR8>(y[3]);
  const float b0 = (c & 8) ? a2 : a0, b1 = (c & 8) ? a3 : a1;
  const float e0 = b0 + dppf<SHL4>(b0), e1 = b1 + dppf<SHR4>(b1);
  float s = (c & 4) ? e1 : e0;
  s += dppf<XOR1>(s);
  return rbf(rbf(s) * SCALE) * LOG2E;
}
template <int N> __device__ __forceinline__ f32x4 pv_step(u32 pp, const u32 (&msk)[4], v4u v, f32x4 acc) {
  v4u a;
  a[(N + 0) & 3] = pp & msk[(N + 0) & 3];
  a[(N + 1) & 3] = dppu<ROR4>(pp) & msk[(N + 1) & 3];
  a[(N + 2) & 3] = dppu<ROR8>(pp) & msk[(N + 2) & 3];
  a[(N + 3) & 3] = dppu<ROR12>(pp) & msk[(N + 3) & 3];
  return mfma(a, v, acc);
}

__device__ __forceinline__ void merge_publish(LAS char* L, int buf, int h, int lane, rsrc_t rw, int team, int slot, u32 tag) {
  const LAS f32x4* ml = (const LAS f32x4*)(L + L_PML + buf * 128 + h * 64);
  const LAS f32x2* pa = (const LAS f32x2*)(L + L_PART + buf * 8192 + h * 512) + lane;
  f32x4 q[4]; f32x2 a[8];
#pragma unroll
  for (int i = 0; i < 4; ++i) q[i] = ml[i];
#pragma unroll
  for (int w = 0; w < 8; ++w) a[w] = pa[w * 128];
  const float M = fmaxf(fmaxf(fmaxf(q[0][0], q[0][2]), fmaxf(q[1][0], q[1][2])), fmaxf(fmaxf(q[2][0], q[2][2]), fmaxf(q[3][0], q[3][2])));
  float Ls = 0.f, o0 = 0.f, o1 = 0.f;
#pragma unroll
  for (int w = 0; w < 8; ++w) {
    const float wt = __builtin_amdgcn_exp2f(q[w >> 1][(w & 1) * 2] - M);
    Ls = fmaf(wt, q[w >> 1][(w & 1) * 2 + 1], Ls); o0 = fmaf(wt, a[w][0], o0); o1 = fmaf(wt, a[w][1], o1);
  }
  put_piece(rw, WS_ACC + ((team * 10 + slot) * 2 + h) * 1024 + lane * 16, asu(o0), asu(o1), tag);
  if (lane == 0) put_piece(rw, WS_ML + (team * 2 + h) * 256 + slot * 16, asu(M), asu(Ls), tag);
}

__global__ void __launch_bounds__(576, 3)
k_fused(const u16* __restrict__ x, const u16* __restrict__ kA, const u16* __restrict__ vA, const u16* __restrict__ wqA, const u16* __restrict__ woA,
        const u16* __restrict__ kB, const u16* __restrict__ vB, const u16* __restrict__ wqB, const u16* __restrict__ woB,
        const u16* __restrict__ gw, u16* __restrict__ out, char* ws) {
  __shared__ __attribute__((aligned(16))) char lds[L_END];
  LAS char* L = (LAS char*)lds;
  const int tid = threadIdx.x, lane = tid & 63, wave = __builtin_amdgcn_readfirstlane(tid >> 6);
  const int wg = blockIdx.x, m = wg & 7, team = wg >> 3, g = team >> 1, b = team & 1;
  const int j = 2 * m + b;
  const bool isA = m >= 6;
  const int qbr = m < 4 ? 1 : 0;
  const int th = m >> 2, tdh = (m >> 1) & 1, tcb = j & 3;
  STAMP(0);
  const rsrc_t rw = mkrs(ws, WS_END);
  const u32 tag = *(const u32*)(ws + WS_GEN) + 1u;

  const rsrc_t rq = mkrs((qbr ? wqB : wqA) + g * 256 + (j & 7) * 32, (DM * NQ - g * 256 - (j & 7) * 32) * 2);
  const int qrow = wave * 64 + (lane >> 2) * 4;
  v4u wq[4];
#pragma unroll
  for (int i = 0; i < 4; ++i) { wq[i] = __builtin_amdgcn_raw_buffer_load_b128(rq, qrow * (NQ * 2) + (lane & 3) * 16, i * (NQ * 2), AUX_NT); SB(); }
  const v2u x0 = *(const v2u*)(x + qrow), x1 = *(const v2u*)(x + DM + qrow);
  SB();
  const int woc = tid % 18, wor = tid / 18;
  const int wodim = 8 * ((wor >> 1) & 15) + 4 * tdh + (wor & 1);
  const int wooff = (((2 * g + th) * DH + wodim) * DM + tcb * 144 + woc * 8) * 2;
  const rsrc_t roA = mkrs(woA, NQ * DM * 2), roB = mkrs(woB, NQ * DM * 2);
  v4u wo[4];
#pragma unroll
  for (int i = 0; i < 4; ++i) { wait_vm<5>(); SB(); wo[i] = __builtin_amdgcn_raw_buffer_load_b128(i < 2 ? roA : roB, wooff, (i & 1) * 2 * DM * 2, AUX_NT); SB(); }
  {
    float s[16];
#pragma unroll
    for (int k = 0; k < 4; ++k) {
      const u32 l01 = __builtin_amdgcn_perm(wq[1][k], wq[0][k], 0x05040100u), h01 = __builtin_amdgcn_perm(wq[1][k], wq[0][k], 0x07060302u);
      const u32 l23 = __builtin_amdgcn_perm(wq[3][k], wq[2][k], 0x05040100u), h23 = __builtin_amdgcn_perm(wq[3][k], wq[2][k], 0x07060302u);
      s[2 * k] = dot2(l23, x0[1], dot2(l01, x0[0], 0.f)); s[2 * k + 1] = dot2(h23, x0[1], dot2(h01, x0[0], 0.f));
      s[8 + 2 * k] = dot2(l23, x1[1], dot2(l01, x1[0], 0.f)); s[8 + 2 * k + 1] = dot2(h23, x1[1], dot2(h01, x1[0], 0.f));
    }
#pragma unroll
    for (int i = 0; i < 8; ++i) { auto t = __builtin_amdgcn_permlane32_swap(asu(s[i]), asu(s[i + 8]), false, false); s[i] = asf(t[0]) + asf(t[1]); }
#pragma unroll
    for (int i = 0; i < 4; ++i) { auto t = __builtin_amdgcn_permlane16_swap(asu(s[i]), asu(s[i + 4]), false, false); s[i] = asf(t[0]) + asf(t[1]); }
    const float a0 = s[0] + dppf<ROR8>(s[0]), a1 = s[1] + dppf<ROR8>(s[1]), a2 = s[2] + dppf<ROR8>(s[2]), a3 = s[3] + dppf<ROR8>(s[3]);
    const float b0 = (lane & 8) ? a2 : a0, b1 = (lane & 8) ? a3 : a1;
    const float e0 = b0 + dppf<SHL4>(b0), e1 = b1 + dppf<SHR4>(b1);
    const float r = (lane & 4) ? e1 : e0;
    ((LAS float*)(L + L_QP))[wave * 64 + (lane >> 5) * 32 + (lane & 3) * 8 + ((lane >> 2) & 7)] = r;
  }
  wg_barrier();
  STAMP(1);
  LAS char* wodst = L + L_WO + wor * WOSTR + woc * 16;

  if (wave == 8) {
    __builtin_amdgcn_s_setprio(3);
    {
      float s = 0.f;
#pragma unroll
      for (int w = 0; w < 9; ++w) s += ((LAS float*)(L + L_QP))[w * 64 + lane];
      const u32 pk = cvtpk(s, dppf<XOR1>(s));
      const u32 pk2 = dppu<XOR2>(pk);
      if ((lane & 3) == 0) put_piece(rw, WS_Q + ((g * 2 + qbr) * 2 + (lane >> 5)) * 1024 + ((j & 7) * 8 + ((lane & 31) >> 2)) * 16, pk, pk2, tag);
    }
    STAMP(2);
#pragma unroll
    for (int i = 0; i < 4; ++i) *(LAS v4u*)(wodst + i * 32 * WOSTR) = wo[i];
    {
      const int off[2] = {WS_Q + ((g * 2 + 1) * 2 + b) * 1024 + lane * 16, WS_Q + ((g * 2 + (isA ? 0 : 1)) * 2 + b) * 1024 + lane * 16};
      v4u v[2];
      get_pieces<2, 2>(rw, off, tag, v);
      *(LAS v2u*)(L + L_QS + 512 + lane * 8) = v2u{v[0][0], v[0][2]};
      *(LAS v2u*)(L + L_QS + lane * 8) = v2u{v[1][0], v[1][2]};
    }
    STAMP(3);
    wg_barrier();
    {
      const int gb = lane >> 5, gl = lane & 31;
      float z = 0.f;
#pragma unroll
      for (int i = 0; i < 9; ++i) {
        const u32 xv = *(const u32*)(x + gb * DM + 2 * gl + 64 * i), gv = *(const u32*)(gw + 2 * gl + 64 * i);
        z += rbf(asf(xv << 16) * asf(gv << 16)) + rbf(asf(xv & 0xffff0000u) * asf(gv & 0xffff0000u));
      }
      z += dppf<XOR1>(z); z += dppf<XOR2>(z); z += dppf<ROR4>(z); z += dppf<ROR8>(z);
      auto t = __builtin_amdgcn_permlane16_swap(asu(z), asu(z), false, false);
      z = rbf(asf(t[0]) + asf(t[1]));
      const float gt = rbf(1.f / (1.f + __expf(-z)));
      if (gl == 0) *(LAS f32x2*)(L + L_G + gb * 8) = f32x2{gt, rbf(1.f - gt)};
    }
    if (isA) {
      wg_barrier();
      merge_publish(L, 0, 0, lane, rw, team, 8 + (m - 6), tag);
      merge_publish(L, 0, 1, lane, rw, team, 8 + (m - 6), tag);
    }
    {
      const int tb = 2 * g + (lane >> 5);
      const int oa = WS_ACC + ((tb * 10 + 8) * 2 + th) * 1024 + (32 * tdh + (lane & 31)) * 16, om = WS_ML + (tb * 2 + th) * 256 + 8 * 16;
      const int off[4] = {oa, oa + 2048, om, om + 16};
      v4u v[4];
      get_pieces<4, 32>(rw, off, tag, v);
      const float m0 = asf(v[2][0]), m1 = asf(v[3][0]), M = fmaxf(m0, m1);
      const float w0 = __builtin_amdgcn_exp2f(m0 - M), w1 = __builtin_amdgcn_exp2f(m1 - M);
      const float inv = 1.f / (w0 * asf(v[2][2]) + w1 * asf(v[3][2]));
      ((LAS u32*)(L + L_OS))[lane] = cvtpk((w0 * asf(v[0][0]) + w1 * asf(v[1][0])) * inv, (w0 * asf(v[0][2]) + w1 * asf(v[1][2])) * inv);
    }
    STAMP(4);
    wg_barrier();
  } else {
    const size_t seg = (size_t)(b * HKV + g) * SK * DH;
    const int key1 = isA ? (SK - WA) + (m - 6) * 1024 : m * 1024;
    const rsrc_t rk1 = mkrs((isA ? kA : kB) + seg + (size_t)key1 * DH, 1024 * 256), rv1 = mkrs((isA ? vA : vB) + seg + (size_t)key1 * DH, 1024 * 256);
    const rsrc_t rk2 = mkrs(kB + seg + (size_t)(6144 + 256 * m) * DH, 256 * 256), rv2 = mkrs(vB + seg + (size_t)(6144 + 256 * m) * DH, 256 * 256);
    const int vo = lane * 16 + wave * GG * 1024;
    const int c = lane & 15;
    constexpr int NT = 80;
    v4u ld[NT];
#define LIDX(i) ((i) < 64 ? ((i) & 31) : ((i) & 7))
#define ISSUE(i) do { if ((i) < NT) { ld[(i) < NT ? (i) : 0] = __builtin_amdgcn_raw_buffer_load_b128((i) < 32 ? rk1 : (i) < 64 ? rv1 : (i) < 72 ? rk2 : rv2, vo, \
                        ((LIDX(i) / GG) * 8 * GG + LIDX(i) % GG) * 1024, AUX_NT); SB(); } } while (0)
#pragma unroll
    for (int i = 0; i < PRE; ++i) {
      wait_vm<FL - 1>(); SB();
      ISSUE(i);
      if (i < 4) { *(LAS v4u*)(wodst + i * 32 * WOSTR) = wo[i]; SB(); }
    }
    STAMP(2);
    wg_barrier();
    STAMP(3);
    const int hown = (c >> 1) & 1;
    v4u qa = *(const LAS v4u*)(L + L_QS + hown * 256 + c * 16), qo = *(const LAS v4u*)(L + L_QS + (hown ^ 1) * 256 + c * 16);
    float sc[8];
    {
      int dummy = 0; (void)dummy;
#define KTILE(t, I0, I1) do { float y[4]; \
        _Pragma("unroll") for (int n = 0; n < 4; ++n) { y[n] = qk_part(ld[4 * (t) + n], qa, qo); SB(); \
          if ((I0) + n < (I1)) { wait_vm<FL - 1>(); SB(); ISSUE((I0) + n); } } \
        sc[t] = qk_red(y, c); SB(); } while (0)
#define IP(t) ((t) < 0 ? PRE : ((PRE + (t) + 1) > (4 * (t) + 4 + FL) ? (PRE + (t) + 1) : (4 * (t) + 4 + FL)))
      KTILE(0, IP(-1), IP(0)); KTILE(1, IP(0), IP(1)); KTILE(2, IP(1), IP(2)); KTILE(3, IP(2), IP(3));
      KTILE(4, IP(3), IP(4)); KTILE(5, IP(4), IP(5)); KTILE(6, IP(5), IP(6)); KTILE(7, IP(6), IP(7));
    }
    static_assert(IP(7) == 32 + FL && IP(7) - IP(6) <= 4 && IP(6) - IP(5) <= 4, "schedule");
    float mx = fmaxf(fmaxf(fmaxf(sc[0], sc[1]), fmaxf(sc[2], sc[3])), fmaxf(fmaxf(sc[4], sc[5]), fmaxf(sc[6], sc[7])));
    mx = fmaxf(mx, dppf<ROR4>(mx)); mx = fmaxf(mx, dppf<ROR8>(mx)); mx = xrow_max(mx);
    float l = 0.f; u32 pp[8];
#pragma unroll
    for (int t = 0; t < 8; ++t) { const float p = __builtin_amdgcn_exp2f(sc[t] - mx); l += p; pp[t] = cvtpk(p, p); }
    const u32 half = (c & 1) ? 0xffff0000u : 0x0000ffffu;
    u32 msk[4];
#pragma unroll
    for (int k = 0; k < 4; ++k) msk[k] = ((c >> 2) == k) ? half : 0u;
    f32x4 acc = {0.f, 0.f, 0.f, 0.f};
    SB();
    STAMP(4);
#define VTILE(t, base, P) do { acc = pv_step<0>(P, msk, ld[(base) + 4 * (t) + 0], acc); SB(); ISSUE((base) + 4 * (t) + 0 + FL); \
      acc = pv_step<1>(P, msk, ld[(base) + 4 * (t) + 1], acc); SB(); ISSUE((base) + 4 * (t) + 1 + FL); \
      acc = pv_step<2>(P, msk, ld[(base) + 4 * (t) + 2], acc); SB(); ISSUE((base) + 4 * (t) + 2 + FL); \
      acc = pv_step<3>(P, msk, ld[(base) + 4 * (t) + 3], acc); SB(); ISSUE((base) + 4 * (t) + 3 + FL); } while (0)
    VTILE(0, 32, pp[0]); VTILE(1, 32, pp[1]); VTILE(2, 32, pp[2]); VTILE(3, 32, pp[3]);
    VTILE(4, 32, pp[4]); VTILE(5, 32, pp[5]); VTILE(6, 32, pp[6]); VTILE(7, 32, pp[7]);
    STAMP(5);
    if (isA) {
      float lt = l; lt += dppf<ROR4>(lt); lt += dppf<ROR8>(lt); lt = xrow_sum(lt);
      *(LAS f32x2*)(L + L_PART + wave * 1024 + lane * 8) = f32x2{acc[0], acc[1]};
      *(LAS f32x2*)(L + L_PART + wave * 1024 + 512 + lane * 8) = f32x2{acc[2], acc[3]};
      if ((lane & ~2) == 0) *(LAS f32x2*)(L + L_PML + (lane >> 1) * 64 + wave * 8) = f32x2{mx, lt};
      wg_barrier();
      mx = -INFINITY; l = 0.f; acc = f32x4{0.f, 0.f, 0.f, 0.f};
      qa = *(const LAS v4u*)(L + L_QS + 512 + hown * 256 + c * 16); qo = *(const LAS v4u*)(L + L_QS + 512 + (hown ^ 1) * 256 + c * 16);
    }
    float s2[2];
#pragma unroll
    for (int t = 0; t < 2; ++t) {
      float y[4];
#pragma unroll
      for (int n = 0; n < 4; ++n) { y[n] = qk_part(ld[64 + 4 * t + n], qa, qo); SB(); ISSUE(64 + 4 * t + n + FL); }
      s2[t] = qk_red(y, c); SB();
    }
    float m2 = fmaxf(s2[0], s2[1]);
    m2 = fmaxf(m2, dppf<ROR4>(m2)); m2 = fmaxf(m2, dppf<ROR8>(m2)); m2 = fmaxf(xrow_max(m2), mx);
    const float al = __builtin_amdgcn_exp2f(mx - m2);
    const float p0 = __builtin_amdgcn_exp2f(s2[0] - m2), p1 = __builtin_amdgcn_exp2f(s2[1] - m2);
    l = fmaf(l, al, p0 + p1);
    const float al0 = dppf<0x00>(al), al1 = dppf<0xAA>(al);
    acc[0] *= al0; acc[1] *= al0; acc[2] *= al1; acc[3] *= al1;
    const u32 pq0 = cvtpk(p0, p0), pq1 = cvtpk(p1, p1);
    l += dppf<ROR4>(l); l += dppf<ROR8>(l); l = xrow_sum(l);
    if ((lane & ~2) == 0) *(LAS f32x2*)(L + L_PML + 128 + (lane >> 1) * 64 + wave * 8) = f32x2{m2, l};
    SB();
    VTILE(0, 72, pq0); VTILE(1, 72, pq1);
    *(LAS f32x2*)(L + L_PART + 8192 + wave * 1024 + lane * 8) = f32x2{acc[0], acc[1]};
    *(LAS f32x2*)(L + L_PART + 8192 + wave * 1024 + 512 + lane * 8) = f32x2{acc[2], acc[3]};
    STAMP(6);
    wg_barrier();
    if (wave < 2) merge_publish(L, 1, wave, lane, rw, team, m, tag);
    STAMP(7);
    {
      const int tb = 2 * g + (wave >> 2), s = lane >> 3;
      const int off[2] = {WS_ACC + ((tb * 10 + s) * 2 + th) * 1024 + (32 * tdh + 8 * (wave & 3) + (lane & 7)) * 16, WS_ML + (tb * 2 + th) * 256 + s * 16};
      v4u v[2];
      get_pieces<2, 0>(rw, off, tag, v);
      STAMP(8);
      const float ms = asf(v[1][0]);
      const float M = xrow_max(fmaxf(ms, dppf<ROR8>(ms)));
      const float wt = __builtin_amdgcn_exp2f(ms - M);
      float Ls = wt * asf(v[1][2]), o0 = wt * asf(v[0][0]), o1 = wt * asf(v[0][2]);
      Ls += dppf<ROR8>(Ls); o0 += dppf<ROR8>(o0); o1 += dppf<ROR8>(o1);
      Ls = xrow_sum(Ls); o0 = xrow_sum(o0); o1 = xrow_sum(o1);
      const float inv = 1.f / Ls;
      if (lane < 8) ((LAS u32*)(L + L_OS + 256))[wave * 8 + lane] = cvtpk(o0 * inv, o1 * inv);
    }
  }
  v4u bf[4];
  {
    const LAS char* p = L + L_WO + (8 * (lane >> 4) + ((lane >> 2) & 3)) * WOSTR + (wave * 16 + (lane & 3) * 4) * 2;
#pragma unroll
    for (int s = 0; s < 4; ++s) {
      const v2u lo = __builtin_bit_cast(v2u, __builtin_amdgcn_ds_read_tr16_b64_v4i16((LAS v4s*)(p + s * 32 * WOSTR)));
      const v2u hi = __builtin_bit_cast(v2u, __builtin_amdgcn_ds_read_tr16_b64_v4i16((LAS v4s*)(p + (s * 32 + 4) * WOSTR)));
      bf[s] = v4u{lo[0], lo[1], hi[0], hi[1]};
    }
  }
  wg_barrier();
  STAMP(9);
  {
    const LAS char* p = L + L_OS + (lane & 1) * 128 + (lane >> 4) * 16;
    f32x4 da = {0.f, 0.f, 0.f, 0.f}, db = {0.f, 0.f, 0.f, 0.f};
    da = mfma(*(const LAS v4u*)(p), bf[0], da); da = mfma(*(const LAS v4u*)(p + 64), bf[1], da);
    db = mfma(*(const LAS v4u*)(p + 256), bf[2], db); db = mfma(*(const LAS v4u*)(p + 320), bf[3], db);
    const f32x4 gt = *(const LAS f32x4*)(L + L_G);
    const float y0 = gt[0] * da[0] + gt[1] * db[0], y1 = gt[2] * da[1] + gt[3] * db[1];
    const int col = tcb * 144 + wave * 16 + lane;
    if (lane < 16) put_piece(rw, WS_Y + (col >> 2) * 4096 + (4 * g + (m >> 1)) * 64 + (col & 3) * 16, asu(y0), asu(y1), tag);
  }
  STAMP(10);
  if (wave == 8 && wg < 144) {
    const int o = WS_Y + wg * 4096 + lane * 16;
    const int off[4] = {o, o + 1024, o + 2048, o + 3072};
    v4u v[4];
    get_pieces<4, 0>(rw, off, tag, v);
    STAMP(11);
    float y0 = (asf(v[0][0]) + asf(v[1][0])) + (asf(v[2][0]) + asf(v[3][0])), y1 = (asf(v[0][2]) + asf(v[1][2])) + (asf(v[2][2]) + asf(v[3][2]));
    y0 += dppf<ROR4>(y0); y1 += dppf<ROR4>(y1); y0 += dppf<ROR8>(y0); y1 += dppf<ROR8>(y1);
    y0 = xrow_sum(y0); y1 = xrow_sum(y1);
    const float r = (lane & 4) ? y1 : y0;
    if (lane < 8) out[(lane >> 2) * DM + 4 * wg + (lane & 3)] = (u16)(cvtpk(r, r) & 0xffffu);
    if (wg == 0) {
      const u32 tnow = (u32)__builtin_amdgcn_s_memrealtime();
      int lo = 0x7fffffff, hi = -0x7fffffff;
      for (int i = 0; i < 36; ++i) {
        const int e = i * 64 + lane, wv = e % 9;
        const bool use = DIAG > 0 && DIAG < 100 && (DIAGH ? wv == 8 : wv != 8);
        const int off2[2] = {e * 32, e * 32 + (use ? 16 : 0)};
        v4u v2[2];
        get_pieces<2, 0>(mkrs(ws + WS_DG, 1 << 20), off2, tag, v2);
        lo = min(lo, (int)(v2[0][0] - tnow));
        if (use || DIAG == 0) hi = DIAGMIN ? max(hi, -(int)(v2[1][0] - tnow)) : max(hi, (int)(v2[1][0] - tnow));
      }
      for (int k = 32; k; k >>= 1) { lo = min(lo, __shfl_xor(lo, k)); hi = max(hi, __shfl_xor(hi, k)); }
      if (DIAGMIN) hi = -hi;
      if (DIAG == 100) hi = 0;
      const u32 X = (u32)(hi - lo) * DIAGK;
      const u32 t1 = tnow + 1500;
      while ((int)((u32)__builtin_amdgcn_s_memrealtime() - (t1 + X)) < 0) __builtin_amdgcn_s_sleep(1);
    }
    if (wg == 0 && lane == 0) *(u32*)(ws + WS_GEN) = tag;
    STAMP(12);
  }
}

extern "C" void kernel_launch(void* const* d_in, const int* in_sizes, int n_in, void* d_out, int out_size, void* d_ws, size_t ws_size, hipStream_t stream) {
  k_fused<<<256, 576, 0, stream>>>((const u16*)d_in[0], (const u16*)d_in[1], (const u16*)d_in[2], (const u16*)d_in[3], (const u16*)d_in[4],
                                   (const u16*)d_in[6], (const u16*)d_in[7], (const u16*)d_in[8], (const u16*)d_in[9], (const u16*)d_in[10],
                                   (u16*)d_out, (char*)d_ws);
}
```

```cpp
#define DIAG 100
#define DIAGH 0
#define DIAGK 0
#define DIAGMIN 0
#include <hip/hip_runtime.h>
#include <hip/hip_bf16.h>
#include <stdint.h>

typedef unsigned u32;
typedef unsigned short u16;
typedef unsigned v4u __attribute__((ext_vector_type(4)));
typedef unsigned v2u __attribute__((ext_vector_type(2)));
typedef float f32x4 __attribute__((ext_vector_type(4)));
typedef float f32x2 __attribute__((ext_vector_type(2)));
typedef short v4s __attribute__((ext_vector_type(4)));
typedef short bf16x8 __attribute__((ext_vector_type(8)));
typedef __bf16 bf16x2 __attribute__((ext_vector_type(2)));
#define LAS __attribute__((address_space(3)))
#define SB() __builtin_amdgcn_sched_barrier(0)
typedef __amdgpu_buffer_rsrc_t rsrc_t;

constexpr int DM = 576, HKV = 16, SK = 8192, DH = 128, WA = 2048, NQ = 4096;
constexpr float SCALE = 0.088388347648318440f, LOG2E = 1.4426950408889634f;
constexpr int AUX_NT = 2, AUX_SC1 = 16;
#define PRE 24
#define FL 4
#define GG 4
constexpr u32 SPIN_CAP = 1u << 18;

constexpr int WS_GEN = 0;
constexpr int WS_Q = 4096;
constexpr int WS_ML = WS_Q + 16 * 4 * 1024;
constexpr int WS_ACC = WS_ML + 16 * 4 * 256;
constexpr int WS_Y = WS_ACC + 16 * 2 * 10 * 2 * 1024;
constexpr int WS_END = WS_Y + 144 * 4096;
constexpr int WS_DG = 2 << 20;
#define STAMP(k) do { if ((k) == 1 && lane == 0) put_piece(mkrs(ws + WS_DG, 1 << 20), ((wg * 9 + wave) * 2) * 16, (u32)T0_, (u32)T0_, tag); \
    if ((k) == 1 && DIAG == 50 && lane == 0) put_piece(mkrs(ws + WS_DG, 1 << 20), ((wg * 9 + wave) * 2 + 1) * 16, (u32)T1_, (u32)T1_, tag); \
    if ((k) > 0 && (k) == DIAG && lane == 0) { const u32 t_ = (u32)__builtin_amdgcn_s_memrealtime(); \
    put_piece(mkrs(ws + WS_DG, 1 << 20), ((wg * 9 + wave) * 2 + 1) * 16, t_, t_, tag); } } while (0)

constexpr int WOSTR = 304;
constexpr int L_WO = 0;
constexpr int L_PART = L_WO + 128 * WOSTR;
constexpr int L_PML = L_PART + 2 * 8192;
constexpr int L_QP = L_PML + 2 * 128;
constexpr int L_QS = L_QP + 9 * 256;
constexpr int L_OS = L_QS + 1024;
constexpr int L_G = L_OS + 512;
constexpr int L_END = L_G + 16;

__device__ __forceinline__ float dot2(u32 k, u32 q, float acc) {
  return __builtin_amdgcn_fdot2_f32_bf16(__builtin_bit_cast(bf16x2, k), __builtin_bit_cast(bf16x2, q), acc, false);
}
__device__ __forceinline__ u32 cvtpk(float lo, float hi) {
  bf16x2 r; r[0] = (__bf16)lo; r[1] = (__bf16)hi; return __builtin_bit_cast(u32, r);
}
__device__ __forceinline__ float rbf(float x) { return __builtin_bit_cast(float, cvtpk(x, x) & 0xffff0000u); }
template <int CTRL> __device__ __forceinline__ float dppf(float x) {
  return __builtin_bit_cast(float, __builtin_amdgcn_mov_dpp(__builtin_bit_cast(int, x), CTRL, 0xf, 0xf, true));
}
template <int CTRL> __device__ __forceinline__ u32 dppu(u32 x) { return (u32)__builtin_amdgcn_mov_dpp((int)x, CTRL, 0xf, 0xf, true); }
__device__ __forceinline__ float asf(u32 x) { return __builtin_bit_cast(float, x); }
__device__ __forceinline__ u32 asu(float x) { return __builtin_bit_cast(u32, x); }
__device__ __forceinline__ f32x4 mfma(v4u a, v4u b, f32x4 c) {
  return __builtin_amdgcn_mfma_f32_16x16x32_bf16(__builtin_bit_cast(bf16x8, a), __builtin_bit_cast(bf16x8, b), c, 0, 0, 0);
}
__device__ __forceinline__ float xrow_max(float x) {
  auto s = __builtin_amdgcn_permlane16_swap(asu(x), asu(x), false, false);
  x = fmaxf(asf(s[0]), asf(s[1]));
  auto t = __builtin_amdgcn_permlane32_swap(asu(x), asu(x), false, false);
  return fmaxf(asf(t[0]), asf(t[1]));
}
__device__ __forceinline__ float xrow_sum(float x) {
  auto s = __builtin_amdgcn_permlane16_swap(asu(x), asu(x), false, false);
  x = asf(s[0]) + asf(s[1]);
  auto t = __builtin_amdgcn_permlane32_swap(asu(x), asu(x), false, false);
  return asf(t[0]) + asf(t[1]);
}
constexpr int XOR1 = 0xB1, XOR2 = 0x4E, ROR4 = 0x124, ROR8 = 0x128, ROR12 = 0x12C, SHL4 = 0x104, SHR4 = 0x114;
__device__ __forceinline__ rsrc_t mkrs(const void* p, int bytes) { return __builtin_amdgcn_make_buffer_rsrc((void*)p, 0, bytes, 0x00020000); }
template <int N> __device__ __forceinline__ void wait_vm() { __builtin_amdgcn_s_waitcnt(0x0F70 | (N & 15) | ((N >> 4) << 14)); }
__device__ __forceinline__ void wg_barrier() {
  __builtin_amdgcn_fence(__ATOMIC_RELEASE, "workgroup", "local");
  __builtin_amdgcn_s_barrier();
  __builtin_amdgcn_fence(__ATOMIC_ACQUIRE, "workgroup", "local");
}
__device__ __forceinline__ void put_piece(rsrc_t rs, int off, u32 a, u32 b, u32 tag) {
  __builtin_amdgcn_raw_buffer_store_b128(v4u{a, tag, b, tag}, rs, off, 0, AUX_SC1);
}
template <int N, int SLEEP> __device__ __forceinline__ void get_pieces(rsrc_t rs, const int (&off)[N], u32 tag, v4u (&v)[N]) {
  bool ok = false;
#pragma nounroll
  for (u32 spin = 0; spin < SPIN_CAP; ++spin) {
    if (!ok) {
#pragma unroll
      for (int k = 0; k < N; ++k) v[k] = __builtin_amdgcn_raw_buffer_load_b128(rs, off[k], 0, AUX_SC1);
      ok = true;
#pragma unroll
      for (int k = 0; k < N; ++k) ok &= (v[k][1] == tag) & (v[k][3] == tag);
    }
    if (__builtin_amdgcn_ballot_w64(!ok) == 0) break;
    if (SLEEP) __builtin_amdgcn_s_sleep(SLEEP);
    asm volatile("" ::: "memory");
  }
}

__device__ __forceinline__ float qk_part(v4u kk, v4u qa, v4u qo) {
  float xa = 0.f, xo = 0.f;
#pragma unroll
  for (int k = 0; k < 4; ++k) { xa = dot2(kk[k], qa[k], xa); xo = dot2(kk[k], qo[k], xo); }
  return xa + dppf<XOR2>(xo);
}
__device__ __forceinline__ float qk_red(const float (&y)[4], int c) {
  const float a0 = y[0] + dppf<ROR8>(y[0]), a1 = y[1] + dppf<ROR8>(y[1]);
  const float a2 = y[2] + dppf<ROR8>(y[2]), a3 = y[3] + dppf<ROR8>(y[3]);
  const float b0 = (c & 8) ? a2 : a0, b1 = (c & 8) ? a3 : a1;
  const float e0 = b0 + dppf<SHL4>(b0), e1 = b1 + dppf<SHR4>(b1);
  float s = (c & 4) ? e1 : e0;
  s += dppf<XOR1>(s);
  return rbf(rbf(s) * SCALE) * LOG2E;
}
template <int N> __device__ __forceinline__ f32x4 pv_step(u32 pp, const u32 (&msk)[4], v4u v, f32x4 acc) {
  v4u a;
  a[(N + 0) & 3] = pp & msk[(N + 0) & 3];
  a[(N + 1) & 3] = dppu<ROR4>(pp) & msk[(N + 1) & 3];
  a[(N + 2) & 3] = dppu<ROR8>(pp) & msk[(N + 2) & 3];
  a[(N + 3) & 3] = dppu<ROR12>(pp) & msk[(N + 3) & 3];
  return mfma(a, v, acc);
}

__device__ __forceinline__ void merge_publish(LAS char* L, int buf, int h, int lane, rsrc_t rw, int team, int slot, u32 tag) {
  const LAS f32x4* ml = (const LAS f32x4*)(L + L_PML + buf * 128 + h * 64);
  const LAS f32x2* pa = (const LAS f32x2*)(L + L_PART + buf * 8192 + h * 512) + lane;
  f32x4 q[4]; f32x2 a[8];
#pragma unroll
  for (int i = 0; i < 4; ++i) q[i] = ml[i];
#pragma unroll
  for (int w = 0; w < 8; ++w) a[w] = pa[w * 128];
  const float M = fmaxf(fmaxf(fmaxf(q[0][0], q[0][2]), fmaxf(q[1][0], q[1][2])), fmaxf(fmaxf(q[2][0], q[2][2]), fmaxf(q[3][0], q[3][2])));
  float Ls = 0.f, o0 = 0.f, o1 = 0.f;
#pragma unroll
  for (int w = 0; w < 8; ++w) {
    const float wt = __builtin_amdgcn_exp2f(q[w >> 1][(w & 1) * 2] - M);
    Ls = fmaf(wt, q[w >> 1][(w & 1) * 2 + 1], Ls); o0 = fmaf(wt, a[w][0], o0); o1 = fmaf(wt, a[w][1], o1);
  }
  put_piece(rw, WS_ACC + ((team * 10 + slot) * 2 + h) * 1024 + lane * 16, asu(o0), asu(o1), tag);
  if (lane == 0) put_piece(rw, WS_ML + (team * 2 + h) * 256 + slot * 16, asu(M), asu(Ls), tag);
}

__global__ void __launch_bounds__(576, 3)
k_fused(const u16* __restrict__ x, const u16* __restrict__ kA, const u16* __restrict__ vA, const u16* __restrict__ wqA, const u16* __restrict__ woA,
        const u16* __restrict__ kB, const u16* __restrict__ vB, const u16* __restrict__ wqB, const u16* __restrict__ woB,
        const u16* __restrict__ gw, u16* __restrict__ out, char* ws) {
  unsigned long long T0_;
  asm volatile("s_memrealtime %0" : "=s"(T0_));
  __shared__ __attribute__((aligned(16))) char lds[L_END];
  LAS char* L = (LAS char*)lds;
  const int tid = threadIdx.x, lane = tid & 63, wave = __builtin_amdgcn_readfirstlane(tid >> 6);
  const int wg = blockIdx.x, m = wg & 7, team = wg >> 3, g = team >> 1, b = team & 1;
  const int j = 2 * m + b;
  const bool isA = m >= 6;
  const int qbr = m < 4 ? 1 : 0;
  const int th = m >> 2, tdh = (m >> 1) & 1, tcb = j & 3;
  unsigned long long T1_;
  asm volatile("s_waitcnt lgkmcnt(0)\n\ts_memrealtime %0\n\ts_waitcnt lgkmcnt(0)" : "=s"(T1_) : "s"(ws), "s"(wqA), "s"(wqB), "s"(T0_));
  const rsrc_t rw = mkrs(ws, WS_END);
  const u32 tag = *(const u32*)(ws + WS_GEN) + 1u;

  const rsrc_t rq = mkrs((qbr ? wqB : wqA) + g * 256 + (j & 7) * 32, (DM * NQ - g * 256 - (j & 7) * 32) * 2);
  const int qrow = wave * 64 + (lane >> 2) * 4;
  v4u wq[4];
#pragma unroll
  for (int i = 0; i < 4; ++i) { wq[i] = __builtin_amdgcn_raw_buffer_load_b128(rq, qrow * (NQ * 2) + (lane & 3) * 16, i * (NQ * 2), AUX_NT); SB(); }
  const v2u x0 = *(const v2u*)(x + qrow), x1 = *(const v2u*)(x + DM + qrow);
  SB();
  const int woc = tid % 18, wor = tid / 18;
  const int wodim = 8 * ((wor >> 1) & 15) + 4 * tdh + (wor & 1);
  const int wooff = (((2 * g + th) * DH + wodim) * DM + tcb * 144 + woc * 8) * 2;
  const rsrc_t roA = mkrs(woA, NQ * DM * 2), roB = mkrs(woB, NQ * DM * 2);
  v4u wo[4];
#pragma unroll
  for (int i = 0; i < 4; ++i) { wait_vm<5>(); SB(); wo[i] = __builtin_amdgcn_raw_buffer_load_b128(i < 2 ? roA : roB, wooff, (i & 1) * 2 * DM * 2, AUX_NT); SB(); }
  {
    float s[16];
#pragma unroll
    for (int k = 0; k < 4; ++k) {
      const u32 l01 = __builtin_amdgcn_perm(wq[1][k], wq[0][k], 0x05040100u), h01 = __builtin_amdgcn_perm(wq[1][k], wq[0][k], 0x07060302u);
      const u32 l23 = __builtin_amdgcn_perm(wq[3][k], wq[2][k], 0x05040100u), h23 = __builtin_amdgcn_perm(wq[3][k], wq[2][k], 0x07060302u);
      s[2 * k] = dot2(l23, x0[1], dot2(l01, x0[0], 0.f)); s[2 * k + 1] = dot2(h23, x0[1], dot2(h01, x0[0], 0.f));
      s[8 + 2 * k] = dot2(l23, x1[1], dot2(l01, x1[0], 0.f)); s[8 + 2 * k + 1] = dot2(h23, x1[1], dot2(h01, x1[0], 0.f));
    }
#pragma unroll
    for (int i = 0; i < 8; ++i) { auto t = __builtin_amdgcn_permlane32_swap(asu(s[i]), asu(s[i + 8]), false, false); s[i] = asf(t[0]) + asf(t[1]); }
#pragma unroll
    for (int i = 0; i < 4; ++i) { auto t = __builtin_amdgcn_permlane16_swap(asu(s[i]), asu(s[i + 4]), false, false); s[i] = asf(t[0]) + asf(t[1]); }
    const float a0 = s[0] + dppf<ROR8>(s[0]), a1 = s[1] + dppf<ROR8>(s[1]), a2 = s[2] + dppf<ROR8>(s[2]), a3 = s[3] + dppf<ROR8>(s[3]);
    const float b0 = (lane & 8) ? a2 : a0, b1 = (lane & 8) ? a3 : a1;
    const float e0 = b0 + dppf<SHL4>(b0), e1 = b1 + dppf<SHR4>(b1);
    const float r = (lane & 4) ? e1 : e0;
    ((LAS float*)(L + L_QP))[wave * 64 + (lane >> 5) * 32 + (lane & 3) * 8 + ((lane >> 2) & 7)] = r;
  }
  wg_barrier();
  STAMP(1);
  LAS char* wodst = L + L_WO + wor * WOSTR + woc * 16;

  if (wave == 8) {
    __builtin_amdgcn_s_setprio(3);
    {
      float s = 0.f;
#pragma unroll
      for (int w = 0; w < 9; ++w) s += ((LAS float*)(L + L_QP))[w * 64 + lane];
      const u32 pk = cvtpk(s, dppf<XOR1>(s));
      const u32 pk2 = dppu<XOR2>(pk);
      if ((lane & 3) == 0) put_piece(rw, WS_Q + ((g * 2 + qbr) * 2 + (lane >> 5)) * 1024 + ((j & 7) * 8 + ((lane & 31) >> 2)) * 16, pk, pk2, tag);
    }
    STAMP(2);
#pragma unroll
    for (int i = 0; i < 4; ++i) *(LAS v4u*)(wodst + i * 32 * WOSTR) = wo[i];
    {
      const int off[2] = {WS_Q + ((g * 2 + 1) * 2 + b) * 1024 + lane * 16, WS_Q + ((g * 2 + (isA ? 0 : 1)) * 2 + b) * 1024 + lane * 16};
      v4u v[2];
      get_pieces<2, 2>(rw, off, tag, v);
      *(LAS v2u*)(L + L_QS + 512 + lane * 8) = v2u{v[0][0], v[0][2]};
      *(LAS v2u*)(L + L_QS + lane * 8) = v2u{v[1][0], v[1][2]};
    }
    STAMP(3);
    wg_barrier();
    {
      const int gb = lane >> 5, gl = lane & 31;
      float z = 0.f;
#pragma unroll
      for (int i = 0; i < 9; ++i) {
        const u32 xv = *(const u32*)(x + gb * DM + 2 * gl + 64 * i), gv = *(const u32*)(gw + 2 * gl + 64 * i);
        z += rbf(asf(xv << 16) * asf(gv << 16)) + rbf(asf(xv & 0xffff0000u) * asf(gv & 0xffff0000u));
      }
      z += dppf<XOR1>(z); z += dppf<XOR2>(z); z += dppf<ROR4>(z); z += dppf<ROR8>(z);
      auto t = __builtin_amdgcn_permlane16_swap(asu(z), asu(z), false, false);
      z = rbf(asf(t[0]) + asf(t[1]));
      const float gt = rbf(1.f / (1.f + __expf(-z)));
      if (gl == 0) *(LAS f32x2*)(L + L_G + gb * 8) = f32x2{gt, rbf(1.f - gt)};
    }
    if (isA) {
      wg_barrier();
      merge_publish(L, 0, 0, lane, rw, team, 8 + (m - 6), tag);
      merge_publish(L, 0, 1, lane, rw, team, 8 + (m - 6), tag);
    }
    {
      const int tb = 2 * g + (lane >> 5);
      const int oa = WS_ACC + ((tb * 10 + 8) * 2 + th) * 1024 + (32 * tdh + (lane & 31)) * 16, om = WS_ML + (tb * 2 + th) * 256 + 8 * 16;
      const int off[4] = {oa, oa + 2048, om, om + 16};
      v4u v[4];
      get_pieces<4, 32>(rw, off, tag, v);
      const float m0 = asf(v[2][0]), m1 = asf(v[3][0]), M = fmaxf(m0, m1);
      const float w0 = __builtin_amdgcn_exp2f(m0 - M), w1 = __builtin_amdgcn_exp2f(m1 - M);
      const float inv = 1.f / (w0 * asf(v[2][2]) + w1 * asf(v[3][2]));
      ((LAS u32*)(L + L_OS))[lane] = cvtpk((w0 * asf(v[0][0]) + w1 * asf(v[1][0])) * inv, (w0 * asf(v[0][2]) + w1 * asf(v[1][2])) * inv);
    }
    STAMP(4);
    wg_barrier();
  } else {
    const size_t seg = (size_t)(b * HKV + g) * SK * DH;
    const int key1 = isA ? (SK - WA) + (m - 6) * 1024 : m * 1024;
    const rsrc_t rk1 = mkrs((isA ? kA : kB) + seg + (size_t)key1 * DH, 1024 * 256), rv1 = mkrs((isA ? vA : vB) + seg + (size_t)key1 * DH, 1024 * 256);
    const rsrc_t rk2 = mkrs(kB + seg + (size_t)(6144 + 256 * m) * DH, 256 * 256), rv2 = mkrs(vB + seg + (size_t)(6144 + 256 * m) * DH, 256 * 256);
    const int vo = lane * 16 + wave * GG * 1024;
    const int c = lane & 15;
    constexpr int NT = 80;
    v4u ld[NT];
#define LIDX(i) ((i) < 64 ? ((i) & 31) : ((i) & 7))
#define ISSUE(i) do { if ((i) < NT) { ld[(i) < NT ? (i) : 0] = __builtin_amdgcn_raw_buffer_load_b128((i) < 32 ? rk1 : (i) < 64 ? rv1 : (i) < 72 ? rk2 : rv2, vo, \
                        ((LIDX(i) / GG) * 8 * GG + LIDX(i) % GG) * 1024, AUX_NT); SB(); } } while (0)
#pragma unroll
    for (int i = 0; i < PRE; ++i) {
      wait_vm<FL - 1>(); SB();
      ISSUE(i);
      if (i < 4) { *(LAS v4u*)(wodst + i * 32 * WOSTR) = wo[i]; SB(); }
    }
    STAMP(2);
    wg_barrier();
    STAMP(3);
    const int hown = (c >> 1) & 1;
    v4u qa = *(const LAS v4u*)(L + L_QS + hown * 256 + c * 16), qo = *(const LAS v4u*)(L + L_QS + (hown ^ 1) * 256 + c * 16);
    float sc[8];
    {
      int dummy = 0; (void)dummy;
#define KTILE(t, I0, I1) do { float y[4]; \
        _Pragma("unroll") for (int n = 0; n < 4; ++n) { y[n] = qk_part(ld[4 * (t) + n], qa, qo); SB(); \
          if ((I0) + n < (I1)) { wait_vm<FL - 1>(); SB(); ISSUE((I0) + n); } } \
        sc[t] = qk_red(y, c); SB(); } while (0)
#define IP(t) ((t) < 0 ? PRE : ((PRE + (t) + 1) > (4 * (t) + 4 + FL) ? (PRE + (t) + 1) : (4 * (t) + 4 + FL)))
      KTILE(0, IP(-1), IP(0)); KTILE(1, IP(0), IP(1)); KTILE(2, IP(1), IP(2)); KTILE(3, IP(2), IP(3));
      KTILE(4, IP(3), IP(4)); KTILE(5, IP(4), IP(5)); KTILE(6, IP(5), IP(6)); KTILE(7, IP(6), IP(7));
    }
    static_assert(IP(7) == 32 + FL && IP(7) - IP(6) <= 4 && IP(6) - IP(5) <= 4, "schedule");
    float mx = fmaxf(fmaxf(fmaxf(sc[0], sc[1]), fmaxf(sc[2], sc[3])), fmaxf(fmaxf(sc[4], sc[5]), fmaxf(sc[6], sc[7])));
    mx = fmaxf(mx, dppf<ROR4>(mx)); mx = fmaxf(mx, dppf<ROR8>(mx)); mx = xrow_max(mx);
    float l = 0.f; u32 pp[8];
#pragma unroll
    for (int t = 0; t < 8; ++t) { const float p = __builtin_amdgcn_exp2f(sc[t] - mx); l += p; pp[t] = cvtpk(p, p); }
    const u32 half = (c & 1) ? 0xffff0000u : 0x0000ffffu;
    u32 msk[4];
#pragma unroll
    for (int k = 0; k < 4; ++k) msk[k] = ((c >> 2) == k) ? half : 0u;
    f32x4 acc = {0.f, 0.f, 0.f, 0.f};
    SB();
    STAMP(4);
#define VTILE(t, base, P) do { acc = pv_step<0>(P, msk, ld[(base) + 4 * (t) + 0], acc); SB(); ISSUE((base) + 4 * (t) + 0 + FL); \
      acc = pv_step<1>(P, msk, ld[(base) + 4 * (t) + 1], acc); SB(); ISSUE((base) + 4 * (t) + 1 + FL); \
      acc = pv_step<2>(P, msk, ld[(base) + 4 * (t) + 2], acc); SB(); ISSUE((base) + 4 * (t) + 2 + FL); \
      acc = pv_step<3>(P, msk, ld[(base) + 4 * (t) + 3], acc); SB(); ISSUE((base) + 4 * (t) + 3 + FL); } while (0)
    VTILE(0, 32, pp[0]); VTILE(1, 32, pp[1]); VTILE(2, 32, pp[2]); VTILE(3, 32, pp[3]);
    VTILE(4, 32, pp[4]); VTILE(5, 32, pp[5]); VTILE(6, 32, pp[6]); VTILE(7, 32, pp[7]);
    STAMP(5);
    if (isA) {
      float lt = l; lt += dppf<ROR4>(lt); lt += dppf<ROR8>(lt); lt = xrow_sum(lt);
      *(LAS f32x2*)(L + L_PART + wave * 1024 + lane * 8) = f32x2{acc[0], acc[1]};
      *(LAS f32x2*)(L + L_PART + wave * 1024 + 512 + lane * 8) = f32x2{acc[2], acc[3]};
      if ((lane & ~2) == 0) *(LAS f32x2*)(L + L_PML + (lane >> 1) * 64 + wave * 8) = f32x2{mx, lt};
      wg_barrier();
      mx = -INFINITY; l = 0.f; acc = f32x4{0.f, 0.f, 0.f, 0.f};
      qa = *(const LAS v4u*)(L + L_QS + 512 + hown * 256 + c * 16); qo = *(const LAS v4u*)(L + L_QS + 512 + (hown ^ 1) * 256 + c * 16);
    }
    float s2[2];
#pragma unroll
    for (int t = 0; t < 2; ++t) {
      float y[4];
#pragma unroll
      for (int n = 0; n < 4; ++n) { y[n] = qk_part(ld[64 + 4 * t + n], qa, qo); SB(); ISSUE(64 + 4 * t + n + FL); }
      s2[t] = qk_red(y, c); SB();
    }
    float m2 = fmaxf(s2[0], s2[1]);
    m2 = fmaxf(m2, dppf<ROR4>(m2)); m2 = fmaxf(m2, dppf<ROR8>(m2)); m2 = fmaxf(xrow_max(m2), mx);
    const float al = __builtin_amdgcn_exp2f(mx - m2);
    const float p0 = __builtin_amdgcn_exp2f(s2[0] - m2), p1 = __builtin_amdgcn_exp2f(s2[1] - m2);
    l = fmaf(l, al, p0 + p1);
    const float al0 = dppf<0x00>(al), al1 = dppf<0xAA>(al);
    acc[0] *= al0; acc[1] *= al0; acc[2] *= al1; acc[3] *= al1;
    const u32 pq0 = cvtpk(p0, p0), pq1 = cvtpk(p1, p1);
    l += dppf<ROR4>(l); l += dppf<ROR8>(l); l = xrow_sum(l);
    if ((lane & ~2) == 0) *(LAS f32x2*)(L + L_PML + 128 + (lane >> 1) * 64 + wave * 8) = f32x2{m2, l};
    SB();
    VTILE(0, 72, pq0); VTILE(1, 72, pq1);
    *(LAS f32x2*)(L + L_PART + 8192 + wave * 1024 + lane * 8) = f32x2{acc[0], acc[1]};
    *(LAS f32x2*)(L + L_PART + 8192 + wave * 1024 + 512 + lane * 8) = f32x2{acc[2], acc[3]};
    STAMP(6);
    wg_barrier();
    if (wave < 2) merge_publish(L, 1, wave, lane, rw, team, m, tag);
    STAMP(7);
    {
      const int tb = 2 * g + (wave >> 2), s = lane >> 3;
      const int off[2] = {WS_ACC + ((tb * 10 + s) * 2 + th) * 1024 + (32 * tdh + 8 * (wave & 3) + (lane & 7)) * 16, WS_ML + (tb * 2 + th) * 256 + s * 16};
      v4u v[2];
      get_pieces<2, 0>(rw, off, tag, v);
      STAMP(8);
      const float ms = asf(v[1][0]);
      const float M = xrow_max(fmaxf(ms, dppf<ROR8>(ms)));
      const float wt = __builtin_amdgcn_exp2f(ms - M);
      float Ls = wt * asf(v[1][2]), o0 = wt * asf(v[0][0]), o1 = wt * asf(v[0][2]);
      Ls += dppf<ROR8>(Ls); o0 += dppf<ROR8>(o0); o1 += dppf<ROR8>(o1);
      Ls = xrow_sum(Ls); o0 = xrow_sum(o0); o1 = xrow_sum(o1);
      const float inv = 1.f / Ls;
      if (lane < 8) ((LAS u32*)(L + L_OS + 256))[wave * 8 + lane] = cvtpk(o0 * inv, o1 * inv);
    }
  }
  v4u bf[4];
  {
    const LAS char* p = L + L_WO + (8 * (lane >> 4) + ((lane >> 2) & 3)) * WOSTR + (wave * 16 + (lane & 3) * 4) * 2;
#pragma unroll
    for (int s = 0; s < 4; ++s) {
      const v2u lo = __builtin_bit_cast(v2u, __builtin_amdgcn_ds_read_tr16_b64_v4i16((LAS v4s*)(p + s * 32 * WOSTR)));
      const v2u hi = __builtin_bit_cast(v2u, __builtin_amdgcn_ds_read_tr16_b64_v4i16((LAS v4s*)(p + (s * 32 + 4) * WOSTR)));
      bf[s] = v4u{lo[0], lo[1], hi[0], hi[1]};
    }
  }
  wg_barrier();
  STAMP(9);
  {
    const LAS char* p = L + L_OS + (lane & 1) * 128 + (lane >> 4) * 16;
    f32x4 da = {0.f, 0.f, 0.f, 0.f}, db = {0.f, 0.f, 0.f, 0.f};
    da = mfma(*(const LAS v4u*)(p), bf[0], da); da = mfma(*(const LAS v4u*)(p + 64), bf[1], da);
    db = mfma(*(const LAS v4u*)(p + 256), bf[2], db); db = mfma(*(const LAS v4u*)(p + 320), bf[3], db);
    const f32x4 gt = *(const LAS f32x4*)(L + L_G);
    const float y0 = gt[0] * da[0] + gt[1] * db[0], y1 = gt[2] * da[1] + gt[3] * db[1];
    const int col = tcb * 144 + wave * 16 + lane;
    if (lane < 16) put_piece(rw, WS_Y + (col >> 2) * 4096 + (4 * g + (m >> 1)) * 64 + (col & 3) * 16, asu(y0), asu(y1), tag);
  }
  STAMP(10);
  if (wave == 8 && wg < 144) {
    const int o = WS_Y + wg * 4096 + lane * 16;
    const int off[4] = {o, o + 1024, o + 2048, o + 3072};
    v4u v[4];
    get_pieces<4, 0>(rw, off, tag, v);
    STAMP(11);
    float y0 = (asf(v[0][0]) + asf(v[1][0])) + (asf(v[2][0]) + asf(v[3][0])), y1 = (asf(v[0][2]) + asf(v[1][2])) + (asf(v[2][2]) + asf(v[3][2]));
    y0 += dppf<ROR4>(y0); y1 += dppf<ROR4>(y1); y0 += dppf<ROR8>(y0); y1 += dppf<ROR8>(y1);
    y0 = xrow_sum(y0); y1 = xrow_sum(y1);
    const float r = (lane & 4) ? y1 : y0;
    if (lane < 8) out[(lane >> 2) * DM + 4 * wg + (lane & 3)] = (u16)(cvtpk(r, r) & 0xffffu);
    if (wg == 0) {
      const u32 tnow = (u32)__builtin_amdgcn_s_memrealtime();
      int lo = 0x7fffffff, hi = -0x7fffffff;
      for (int i = 0; i < 36; ++i) {
        const int e = i * 64 + lane, wv = e % 9;
        const bool use = DIAG > 0 && DIAG < 100 && (DIAGH ? wv == 8 : wv != 8);
        const int off2[2] = {e * 32, e * 32 + (use ? 16 : 0)};
        v4u v2[2];
        get_pieces<2, 0>(mkrs(ws + WS_DG, 1 << 20), off2, tag, v2);
        lo = min(lo, (int)(v2[0][0] - tnow));
        if (use || DIAG == 0) hi = DIAGMIN ? max(hi, -(int)(v2[1][0] - tnow)) : max(hi, (int)(v2[1][0] - tnow));
      }
      for (int k = 32; k; k >>= 1) { lo = min(lo, __shfl_xor(lo, k)); hi = max(hi, __shfl_xor(hi, k)); }
      if (DIAGMIN) hi = -hi;
      if (DIAG == 100) hi = 0;
      const u32 X = (u32)(hi - lo) * DIAGK;
      const u32 t1 = tnow + 1500;
      while ((int)((u32)__builtin_amdgcn_s_memrealtime() - (t1 + X)) < 0) __builtin_amdgcn_s_sleep(1);
    }
    if (wg == 0 && lane == 0) *(u32*)(ws + WS_GEN) = tag;
    STAMP(12);
  }
}

extern "C" void kernel_launch(void* const* d_in, const int* in_sizes, int n_in, void* d_out, int out_size, void* d_ws, size_t ws_size, hipStream_t stream) {
  k_fused<<<256, 576, 0, stream>>>((const u16*)d_in[0], (const u16*)d_in[1], (const u16*)d_in[2], (const u16*)d_in[3], (const u16*)d_in[4],
                                   (const u16*)d_in[6], (const u16*)d_in[7], (const u16*)d_in[8], (const u16*)d_in[9], (const u16*)d_in[10],
                                   (u16*)d_out, (char*)d_ws);
}
```

```cpp
#include <hip/hip_runtime.h>
#include <hip/hip_bf16.h>
#include <stdint.h>

typedef unsigned u32;
typedef unsigned short u16;
typedef unsigned v4u __attribute__((ext_vector_type(4)));
typedef unsigned v2u __attribute__((ext_vector_type(2)));
typedef float f32x4 __attribute__((ext_vector_type(4)));
typedef float f32x2 __attribute__((ext_vector_type(2)));
typedef short v4s __attribute__((ext_vector_type(4)));
typedef short bf16x8 __attribute__((ext_vector_type(8)));
typedef __bf16 bf16x2 __attribute__((ext_vector_type(2)));
#define LAS __attribute__((address_space(3)))
#define GAS __attribute__((address_space(1)))
#define CAS __attribute__((address_space(4)))
#define SB() __builtin_amdgcn_sched_barrier(0)
#define PIN(v) asm volatile("" : "+v"(v))
#define NEW_CODE_PAGE() asm volatile("s_branch 1f\n\t.p2align 12\n1:")
typedef __amdgpu_buffer_rsrc_t rsrc_t;

constexpr int DM = 576, HKV = 16, SK = 8192, DH = 128, WA = 2048, NQ = 4096;
constexpr float SCALE = 0.088388347648318440f, LOG2E = 1.4426950408889634f;
constexpr int AUX_NT = 2, AUX_SC1 = 16;
constexpr int PRE = 24;
constexpr int FL = 8;
constexpr int QAT = 5;
constexpr int TAT = 14;
constexpr int TAU = 422;
constexpr int TAUP = 320;
constexpr int TAUO = 400;
constexpr int OFFK = 1830;
constexpr int RAMP2 = 23;
constexpr int RAMP3 = 8;
constexpr int GAP = 60;
constexpr int GAPA = 180;
constexpr int GAPB = 80;
constexpr int OFFO = 410;
constexpr int TQPOLL = 460;
constexpr int TMLPOLL = 400;
constexpr int DA = 133;
constexpr int DB = 212;
constexpr int TBIAS = -6;
constexpr int CODELINES = 177;
constexpr u32 SPIN_CAP = 1u << 18;

constexpr int WS_GEN = 0;
constexpr int WS_T = 2048;
constexpr int WS_FL = 4096;
constexpr int WS_Q = WS_FL + 256 * 128;
constexpr int WS_ML = WS_Q + 16 * 4 * 4096;
constexpr int WS_V = WS_ML + 256 * 128;
constexpr int WS_PR0 = 1 << 19;
constexpr int WS_ACC = 1 << 20;
constexpr int WS_Y = 1 << 21;
constexpr int WS_PR1 = WS_Y + 144 * 8192;
constexpr int WS_END = WS_PR1 + 16384;
static_assert(WS_V + 2048 <= WS_PR0, "ws");
__device__ __forceinline__ int two_places(int idx, int side) { return (idx >> 13) * 16384 + (idx & 8191) + ((side & 1) << 13); }

constexpr int WOSTR = 304;
constexpr int L_WO = 0;
constexpr int L_PART = L_WO + 128 * WOSTR;
constexpr int L_PML = L_PART + 2 * 8192;
constexpr int L_F = L_PML + 256;
constexpr int L_QP = L_F + 128;
constexpr int L_QS = L_QP + 9 * 1024;
constexpr int L_OS = L_QS + 1024;
constexpr int L_G = L_OS + 512;
constexpr int L_TC = L_G + 16;
constexpr int L_TS = L_TC + 8;
constexpr int L_SD = L_TC + 16;
constexpr int L_USED = L_TC + 32;
constexpr int L_END = 84 * 1024;
static_assert(L_USED <= L_END, "lds");

__device__ __forceinline__ float dot2(u32 k, u32 q, float acc) {
  return __builtin_amdgcn_fdot2_f32_bf16(__builtin_bit_cast(bf16x2, k), __builtin_bit_cast(bf16x2, q), acc, false);
}
__device__ __forceinline__ u32 cvtpk(float lo, float hi) {
  bf16x2 r; r[0] = (__bf16)lo; r[1] = (__bf16)hi; return __builtin_bit_cast(u32, r);
}
__device__ __forceinline__ float rbf(float x) { return __builtin_bit_cast(float, cvtpk(x, x) & 0xffff0000u); }
template <int CTRL> __device__ __forceinline__ float dppf(float x) {
  return __builtin_bit_cast(float, __builtin_amdgcn_mov_dpp(__builtin_bit_cast(int, x), CTRL, 0xf, 0xf, true));
}
template <int CTRL> __device__ __forceinline__ u32 dppu(u32 x) { return (u32)__builtin_amdgcn_mov_dpp((int)x, CTRL, 0xf, 0xf, true); }
__device__ __forceinline__ float asf(u32 x) { return __builtin_bit_cast(float, x); }
__device__ __forceinline__ u32 asu(float x) { return __builtin_bit_cast(u32, x); }
__device__ __forceinline__ f32x4 mfma(v4u a, v4u b, f32x4 c) {
  return __builtin_amdgcn_mfma_f32_16x16x32_bf16(__builtin_bit_cast(bf16x8, a), __builtin_bit_cast(bf16x8, b), c, 0, 0, 0);
}
__device__ __forceinline__ float xrow_max(float x) {
  auto s = __builtin_amdgcn_permlane16_swap(asu(x), asu(x), false, false);
  x = fmaxf(asf(s[0]), asf(s[1]));
  auto t = __builtin_amdgcn_permlane32_swap(asu(x), asu(x), false, false);
  return fmaxf(asf(t[0]), asf(t[1]));
}
__device__ __forceinline__ float xrow_sum(float x) {
  auto s = __builtin_amdgcn_permlane16_swap(asu(x), asu(x), false, false);
  x = asf(s[0]) + asf(s[1]);
  auto t = __builtin_amdgcn_permlane32_swap(asu(x), asu(x), false, false);
  return asf(t[0]) + asf(t[1]);
}
constexpr int XOR1 = 0xB1, XOR2 = 0x4E, XOR7 = 0x141, ROR4 = 0x124, ROR8 = 0x128, ROR12 = 0x12C, SHL4 = 0x104, SHR4 = 0x114;
__device__ __forceinline__ rsrc_t mkrs(const void* p, int bytes) { return __builtin_amdgcn_make_buffer_rsrc((void*)p, 0, bytes, 0x00020000); }
template <int N> __device__ __forceinline__ void wait_vm() { __builtin_amdgcn_s_waitcnt(0x0F70 | (N & 15) | ((N >> 4) << 14)); }
__device__ __forceinline__ void wg_barrier() {
  __builtin_amdgcn_fence(__ATOMIC_RELEASE, "workgroup", "local");
  __builtin_amdgcn_s_barrier();
  __builtin_amdgcn_fence(__ATOMIC_ACQUIRE, "workgroup", "local");
}
__device__ __forceinline__ void pace(u32 due) {
  asm volatile("1:\n\ts_memrealtime vcc\n\ts_waitcnt lgkmcnt(0)\n\ts_sub_u32 vcc_lo, vcc_lo, %0\n\ts_cmp_lt_i32 vcc_lo, 0\n\ts_cbranch_scc1 1b" :: "s"(__builtin_amdgcn_readfirstlane(due)) : "vcc", "scc");
}
__device__ __forceinline__ void put_piece(rsrc_t rs, int off, u32 a, u32 b, u32 tag) {
  __builtin_amdgcn_raw_buffer_store_b128(v4u{a, tag, b, tag}, rs, off, 0, AUX_SC1);
}
template <int N, int SLEEP> __device__ __forceinline__ void get_pieces(rsrc_t rs, const int (&off)[N], u32 tag, v4u (&v)[N], bool ok = false) {
#pragma nounroll
  for (u32 spin = 0; spin < SPIN_CAP; ++spin) {
    if (!ok) {
#pragma unroll
      for (int k = 0; k < N; ++k) v[k] = __builtin_amdgcn_raw_buffer_load_b128(rs, off[k], 0, AUX_SC1);
      ok = true;
#pragma unroll
      for (int k = 0; k < N; ++k) ok &= (v[k][1] == tag) & (v[k][3] == tag);
    }
    if (__builtin_amdgcn_ballot_w64(!ok) == 0) break;
    if (SLEEP) __builtin_amdgcn_s_sleep(SLEEP);
    asm volatile("" ::: "memory");
  }
}

template <int N> __device__ __forceinline__ bool any_stale(const v4u (&v)[N], u32 tag) {
  u32 bad = 0;
#pragma unroll
  for (int k = 0; k < N; ++k) bad |= (v[k][1] ^ tag) | (v[k][3] ^ tag);
  return __builtin_expect(__builtin_amdgcn_ballot_w64(bad != 0) != 0, 0);
}

__device__ __forceinline__ float qk_part(v4u kk, v4u qa, v4u qo) {
  float xa = 0.f, xo = 0.f;
#pragma unroll
  for (int k = 0; k < 4; ++k) { xa = dot2(kk[k], qa[k], xa); xo = dot2(kk[k], qo[k], xo); }
  return xa + dppf<XOR2>(xo);
}
__device__ __forceinline__ float qk_red(const float (&y)[4], int c) {
  const float a0 = y[0] + dppf<ROR8>(y[0]), a1 = y[1] + dppf<ROR8>(y[1]);
  const float a2 = y[2] + dppf<ROR8>(y[2]), a3 = y[3] + dppf<ROR8>(y[3]);
  const float b0 = (c & 8) ? a2 : a0, b1 = (c & 8) ? a3 : a1;
  const float e0 = b0 + dppf<SHL4>(b0), e1 = b1 + dppf<SHR4>(b1);
  float s = (c & 4) ? e1 : e0;
  s += dppf<XOR1>(s);
  return rbf(rbf(s) * SCALE) * LOG2E;
}
template <int N> __device__ __forceinline__ v4u pv_frag(u32 pp, const u32 (&msk)[4]) {
  v4u a;
  a[(N + 0) & 3] = pp & msk[(N + 0) & 3];
  a[(N + 1) & 3] = dppu<ROR4>(pp) & msk[(N + 1) & 3];
  a[(N + 2) & 3] = dppu<ROR8>(pp) & msk[(N + 2) & 3];
  a[(N + 3) & 3] = dppu<ROR12>(pp) & msk[(N + 3) & 3];
  return a;
}
__device__ __forceinline__ v2u merge8(const LAS char* p, const f32x2 (&f)[8]) {
  f32x4 a[8];
#pragma unroll
  for (int w = 0; w < 8; ++w) a[w] = *(const LAS f32x4*)(p + w * 1024);
  f32x2 s0 = a[0].xy * f[0][0], s1 = a[0].zw * f[4][0];
#pragma unroll
  for (int w = 1; w < 8; ++w) {
    const float f0 = f[w >> 1][w & 1], f1 = f[4 + (w >> 1)][w & 1];
    s0 = __builtin_elementwise_fma(a[w].xy, f32x2{f0, f0}, s0);
    s1 = __builtin_elementwise_fma(a[w].zw, f32x2{f1, f1}, s1);
  }
  return v2u{cvtpk(s0[0], s0[1]), cvtpk(s1[0], s1[1])};
}

__global__ void __launch_bounds__(576, 3) __attribute__((aligned(4096)))
k_fused(const u16*, const u16*, const u16*, const u16*, const u16*, const u16*, const u16*, const u16*, const u16*, const u16*, u16*, char*) {
  typedef unsigned long long v4p __attribute__((ext_vector_type(4)));
  v4p ka0, ka1, ka2;
  asm volatile("s_load_dwordx8 %0, %3, 0x0\n\ts_load_dwordx8 %1, %3, 0x20\n\ts_load_dwordx8 %2, %3, 0x40"
               : "=&s"(ka0), "=&s"(ka1), "=&s"(ka2) : "s"(__builtin_amdgcn_kernarg_segment_ptr()));
  SB();
  __shared__ __attribute__((aligned(16))) char lds[L_END];
  LAS char* L = (LAS char*)lds;
  const int tid = threadIdx.x, lane = tid & 63, wave = __builtin_amdgcn_readfirstlane(tid >> 6);
  const int wg = blockIdx.x, m = (wg >> 3) & 7, b = (wg >> 6) & 1, g = 2 * (wg & 7) + (wg >> 7), team = 2 * g + b;
  const int qbr = b, qh = m >> 2, kq = m & 3;
  const int th = b, tdh = m >> 2, tcb = m & 3;
  const int c = lane & 15;
  unsigned long long pc0;
  asm volatile("s_getpc_b64 %0" : "=s"(pc0));

  const int qrow = kq * 144 + wave * 16 + (lane >> 4);
  int qoff = qrow * (NQ * 2) + (2 * g + qh) * 256 + c * 16;
  PIN(qoff);
  if (tid == 0) *(LAS u32*)(L + L_TS + 4) = 0u;
  const u32 pf = __builtin_amdgcn_raw_buffer_load_b32(mkrs((const char*)pc0 + min((wg >> 3) * 6, CODELINES - 12) * 128, wave == 0 ? 12 * 128 : 0), lane * 128, 0, 0);
  SB();
  asm volatile("s_waitcnt lgkmcnt(0)" : "+s"(ka0), "+s"(ka1), "+s"(ka2));
#define ARG(T, v) ((T*)(GAS T*)(v))
  const u16 *x = ARG(const u16, ka0[0]), *kA = ARG(const u16, ka0[1]), *vA = ARG(const u16, ka0[2]), *wqA = ARG(const u16, ka0[3]);
  const u16 *woA = ARG(const u16, ka1[0]), *kB = ARG(const u16, ka1[1]), *vB = ARG(const u16, ka1[2]), *wqB = ARG(const u16, ka1[3]);
  const u16 *woB = ARG(const u16, ka2[0]), *gw = ARG(const u16, ka2[1]);
  u16* out = ARG(u16, ka2[2]);
  char* ws = ARG(char, ka2[3]);
  const rsrc_t rq = mkrs(qbr ? wqB : wqA, DM * NQ * 2);
  v4u wq[4];
  wq[0] = __builtin_amdgcn_raw_buffer_load_b128(rq, qoff, 0, AUX_NT);
  SB();
  u32 Tb = (u32)__builtin_amdgcn_s_memrealtime();
  const int vb = m | ((wg & 7) << 3) | (b << 6);
#define PAR(mask) (__builtin_popcount(vb & (mask)) & 1)
  const int ph = PAR(56) | (PAR(111) << 1) | (PAR(102) << 2);
  u32 T0 = Tb + ((ph * TAU) >> 7);
  SB();
  wg_barrier();
  const u32 Tq = wave == 8 ? Tb - 1000 : T0;
#pragma unroll
  for (int i = 1; i < 4; ++i) { SB(); pace(Tq + ((i * TAUP) >> 4)); SB(); wq[i] = __builtin_amdgcn_raw_buffer_load_b128(rq, qoff, i * 4 * (NQ * 2), AUX_NT); SB(); }
  u32 gen = 0, t0 = 0, t1 = 0, t2 = 0, t3 = 0, t4 = 0, t5 = 0, t6 = 0, t7 = 0, t8 = 0;
  int wv = wave;
  asm volatile("" : "+s"(wv));
  if (wv == 8) {
    const u32 so = ((b * HKV + g) * SK + SK - 1) * DH * 2, wso = (2 * g + th) * DH * DM * 2;
    gen = *(const CAS u32*)(ws + WS_GEN);
    t0 = *(const CAS u32*)x; t1 = *(const CAS u32*)((const char*)kB + so); t2 = *(const CAS u32*)((const char*)kA + so); t3 = *(const CAS u32*)((const char*)vA + so);
    t4 = *(const CAS u32*)((const char*)vB + so); t5 = *(const CAS u32*)((const char*)woA + wso); t6 = *(const CAS u32*)((const char*)woB + wso);
    t7 = *(const CAS u32*)((const char*)gw + min(wg >> 3, 17) * 64);
    t8 = *(const CAS u32*)(ws + WS_END - 64);
  }
  const int woc = tid % 18, wor = tid / 18;
  const int wooff = (((2 * g + th) * DH + 64 * tdh + wor) * DM + tcb * 144 + woc * 8) * 2;
  const int phw = wg >> 5;
  const rsrc_t rw = mkrs(ws, WS_END);
  const rsrc_t roA = mkrs(woA, NQ * DM * 2), roB = mkrs(woB, NQ * DM * 2);
  const u32 half = (c & 1) ? 0xffff0000u : 0x0000ffffu;
  u32 msk[4];
#pragma unroll
  for (int k = 0; k < 4; ++k) msk[k] = ((c >> 2) == k) ? half : 0u;
  const u32* xp = (const u32*)(x + ((c >> 1) & 1) * DM + (qrow & ~1) + (c >> 2) * 4);
  u32 xv;
#define SLIP(N) do { SB(); wait_vm<N>(); const int late_ = (int)((u32)__builtin_amdgcn_s_memrealtime() - (T0 + (OFFK >> 4))); if (late_ > 0) { T0 += late_; Tb += late_; } SB(); } while (0)
  u32 sides;
  u32 tag, TE, TWA, TWB;
  v4u wo[4];
#define QPART() do { PIN(xv); asm volatile("" :: "v"(pf)); const u32 xx = __builtin_amdgcn_perm(xv, xv, (lane & 16) ? 0x03020302u : 0x01000100u); f32x4 s = {0.f, 0.f, 0.f, 0.f}; \
    s = mfma(pv_frag<0>(xx, msk), wq[0], s); s = mfma(pv_frag<1>(xx, msk), wq[1], s); \
    s = mfma(pv_frag<2>(xx, msk), wq[2], s); s = mfma(pv_frag<3>(xx, msk), wq[3], s); \
    *(LAS f32x4*)(L + L_QP + tid * 16) = s; wg_barrier();   } while (0)
#define TPRE (OFFK + 16 * RAMP2 + 2 * GAP + GAPA)
#define TMID (2 * TAUO + GAPB + 8 * RAMP3)
#define SET_TIMES(Tc) do { TE = (Tc) + ((TPRE + TMID + 79 * TAU + 7 * TAU / 8) >> 4); TWA = (Tc) + ((TPRE + 48 * TAU + phw * TAUO / 8) >> 4); \
    TWB = (Tc) + ((TPRE + TMID + 80 * TAU + OFFO) >> 4); } while (0)
#define WOLD(i) do { SB(); if ((i) < 3) pace((i) < 2 ? TWA + (((i) * TAUO) >> 4) : TWB); SB(); \
    wo[i] = __builtin_amdgcn_raw_buffer_load_b128((i) < 2 ? roA : roB, wooff, ((i) & 1) * 32 * DM * 2, AUX_NT); SB(); } while (0)
#define WOST(i) *(LAS v4u*)(L + L_WO + (wor + (i) * 32) * WOSTR + woc * 16) = wo[i]
  const LAS char* pb = L + L_WO + (8 * (lane >> 4) + ((lane >> 2) & 3)) * WOSTR + (wave * 16 + (lane & 3) * 4) * 2;
#define BFRAG(s) ({ const v2u lo_ = __builtin_bit_cast(v2u, __builtin_amdgcn_ds_read_tr16_b64_v4i16((LAS v4s*)(pb + (s) * 32 * WOSTR))); \
      const v2u hi_ = __builtin_bit_cast(v2u, __builtin_amdgcn_ds_read_tr16_b64_v4i16((LAS v4s*)(pb + ((s) * 32 + 4) * WOSTR))); v4u{lo_[0], lo_[1], hi_[0], hi_[1]}; })
  int yo, pao, pbo, fo = 0, oo = 0;
  const int fq = (wg & 7) * 16 + ((wg >> 3) & 15) + (wg >> 7) * 128;
#define YOFF() do { const int col = tcb * 144 + wave * 16 + lane; \
    yo = lane < 16 ? WS_Y + two_places((col >> 3) * 8192 + (4 * g + 2 * th + tdh) * 128 + (col & 7) * 16, sides >> ((col >> 6) & 7)) : 1 << 30; PIN(yo); \
    pao = L_OS + (lane & 1) * 128 + (lane >> 4) * 16; PIN(pao); pbo = (int)(pb - L); PIN(pbo); } while (0)
  v4u bf0, bf1;
  f32x4 gt;
#define EARLY_READS() do { bf0 = BFRAG(0); bf1 = BFRAG(1); PIN(bf0); PIN(bf1); } while (0)

  if (wave == 8) {
    __builtin_amdgcn_s_setprio(3);
    asm volatile("" :: "s"(t0), "s"(t1), "s"(t2), "s"(t3), "s"(t4), "s"(t5), "s"(t6), "s"(t7), "s"(t8));
    tag = gen + 1u;
    asm volatile("" : "+s"(tag));
    SB();
    xv = *xp;
    for (;;) {
      const v2u t = *(volatile LAS v2u*)(L + L_TS);
      if (__builtin_amdgcn_readfirstlane(t[1])) { const u32 t7s = __builtin_amdgcn_readfirstlane(t[0]); T0 += t7s - Tb; Tb = t7s; break; }
    }
    const u32 die = (__builtin_amdgcn_s_getreg(20 | (3 << 11)) >> 2) & 1;
    const bool rep = (wg & 0x78) == 0x78;
    const int ro = ((wg & 7) | ((wg >> 7) << 3)) * 128 + lane * 16, pro = ((wg >> 7) ? WS_PR1 : WS_PR0) + (wg & 7) * 512;
    put_piece(rw, rep && lane < 8 ? WS_T + ro : 1 << 30, Tb, die, tag);
    QPART();
    {
      f32x4 s = *(const LAS f32x4*)(L + L_QP + lane * 16);
#pragma unroll
      for (int w = 1; w < 9; ++w) s += *(const LAS f32x4*)(L + L_QP + w * 1024 + lane * 16);
      auto t0 = __builtin_amdgcn_permlane16_swap(asu(s[0]), asu(s[2]), false, false);
      auto t1 = __builtin_amdgcn_permlane16_swap(asu(s[1]), asu(s[3]), false, false);
      __builtin_amdgcn_raw_buffer_store_b128(v4u{t0[0], t1[0], t0[1], t1[1]}, rw,
          WS_Q + ((g * 2 + qbr) * 2 + ((lane >> 4) & 1)) * 4096 + (qh * 4 + kq) * 512 + (lane >> 5) * 256 + c * 16, 0, AUX_SC1);
    }
    asm volatile("s_waitcnt vmcnt(0)" ::: "memory");
    if (lane < 8) put_piece(rw, WS_FL + wg * 128 + lane * 16, tag, tag, tag);
    {
      const int off[1] = {WS_T + c * 128};
      v4u v[1];
      get_pieces<1, 16>(rw, off, tag, v);
      float d = (float)(int)(v[0][0] - Tb), e = (v[0][2] & 1) ? d : -d;
      d += dppf<XOR1>(d); d += dppf<XOR2>(d); d += dppf<ROR4>(d); d += dppf<ROR8>(d);
      e += dppf<XOR1>(e); e += dppf<XOR2>(e); e += dppf<ROR4>(e); e += dppf<ROR8>(e);
      const u32 Tc = Tb + (u32)__builtin_amdgcn_readfirstlane((int)floorf(fminf(fmaxf(d * (1.f / 16.f) + fabsf(e) * (1.f / 16.f), -300.f), 300.f))) + TBIAS;
      if (lane == 0) *(LAS v2u*)(L + L_TC) = v2u{Tc, tag};
      SET_TIMES(Tc);
    }
    wg_barrier();
    if (rep) put_piece(rw, pro + (lane >> 5) * 8192 + (lane & 31) * 16, 0u, 0u, 0u);
    {
      const int off[1] = {WS_FL + ((wg & 0x87) | (c << 3)) * 128};
      v4u v[1];
      pace(T0 + TQPOLL);
      get_pieces<1, 16>(rw, off, tag, v);
      __builtin_amdgcn_fence(__ATOMIC_ACQUIRE, "wavefront");
    }
#pragma unroll
    for (int br = 1; br >= 0; --br) {
      const int o = WS_Q + ((g * 2 + br) * 2 + b) * 4096 + (lane >> 5) * 2048 + (lane & 31) * 16;
      f32x4 v[4];
#pragma unroll
      for (int k = 0; k < 4; ++k) v[k] = __builtin_bit_cast(f32x4, __builtin_amdgcn_raw_buffer_load_b128(rw, o + k * 512, 0, AUX_SC1));
      const f32x4 s = (v[0] + v[1]) + (v[2] + v[3]);
      *(LAS v2u*)(L + L_QS + br * 512 + (lane >> 5) * 256 + c * 16 + ((lane >> 4) & 1) * 8) = v2u{cvtpk(s[0], s[1]), cvtpk(s[2], s[3])};
      if (br) wg_barrier();
    }
    if (rep) {
      u32 lat[2] = {~0u, ~0u};
#pragma unroll
      for (int k = 0; k < 8; ++k) {
        unsigned long long ta, tb; u32 dummy;
        asm volatile("s_memtime %0\n\ts_waitcnt lgkmcnt(0)\n\ts_load_dword %2, %3, %4\n\ts_waitcnt lgkmcnt(0)\n\ts_memtime %1\n\ts_waitcnt lgkmcnt(0)"
                     : "=&s"(ta), "=&s"(tb), "=&s"(dummy) : "s"(ws + pro), "i"((k & 1) * 8192 + (k >> 1) * 128));
        lat[k & 1] = min(lat[k & 1], (u32)(tb - ta));
      }
      const int d = min(max((int)(lat[1] - lat[0]), -400), 400);
      if (lane < 8) put_piece(rw, WS_V + ro, asu((float)(die ? -d : d)), die, tag);
    }
    wg_barrier();
    WOLD(0); WOLD(1);
    wg_barrier();
    {
      const f32x2 ml = *(const LAS f32x2*)(L + L_PML + (lane & 31) * 8);
      float M = ml[0];
      M = fmaxf(M, dppf<XOR1>(M)); M = fmaxf(M, dppf<XOR2>(M)); M = fmaxf(M, dppf<XOR7>(M));
      float Ls = ml[1] * __builtin_amdgcn_exp2f(ml[0] - M);
      Ls += dppf<XOR1>(Ls); Ls += dppf<XOR2>(Ls); Ls += dppf<XOR7>(Ls);
      if ((lane & 7) == 0) put_piece(rw, WS_ML + (team * 8 + m) * 128 + lane * 2, asu(M), asu(Ls), tag);
      const u32 tml = (u32)__builtin_amdgcn_s_memrealtime() + TMLPOLL;
      WOST(0); WOST(1);
      wg_barrier();
      {
        const rsrc_t r0 = mkrs(x, DM * 2), r1 = mkrs(x + DM, DM * 2), r2 = mkrs(gw, DM * 2);
        v4u gx[6];
#pragma unroll
        for (int i = 0; i < 6; ++i) gx[i] = __builtin_amdgcn_raw_buffer_load_b128(i < 2 ? r0 : i < 4 ? r1 : r2, lane * 16 + (i & 1) * 1024, 0, 0);
        float z0 = 0.f, z1 = 0.f;
#pragma unroll
        for (int i = 0; i < 8; ++i) {
          const u32 gv = gx[4 + (i >> 2)][i & 3], a0 = gx[i >> 2][i & 3], a1 = gx[2 + (i >> 2)][i & 3];
          z0 += rbf(asf(a0 << 16) * asf(gv << 16)) + rbf(asf(a0 & 0xffff0000u) * asf(gv & 0xffff0000u));
          z1 += rbf(asf(a1 << 16) * asf(gv << 16)) + rbf(asf(a1 & 0xffff0000u) * asf(gv & 0xffff0000u));
        }
        auto t32 = __builtin_amdgcn_permlane32_swap(asu(z0), asu(z1), false, false);
        float z = asf(t32[0]) + asf(t32[1]);
        z += dppf<XOR1>(z); z += dppf<XOR2>(z); z += dppf<ROR4>(z); z += dppf<ROR8>(z);
        auto t = __builtin_amdgcn_permlane16_swap(asu(z), asu(z), false, false);
        z = rbf(asf(t[0]) + asf(t[1]));
        const float gt = rbf(1.f / (1.f + __expf(-z)));
        if ((lane & 31) == 0) *(LAS f32x2*)(L + L_G + (lane >> 5) * 8) = f32x2{gt, rbf(1.f - gt)};
      }
      const int off[1] = {lane < 32 ? WS_ML + (team * 8 + (lane & 7)) * 128 + (lane & 24) * 2 : WS_V + c * 128};
      v4u v[1];
      NEW_CODE_PAGE();
      pace(tml);
      get_pieces<1, 32>(rw, off, tag, v);
      if (wg == 0 && lane == 0) __builtin_amdgcn_raw_buffer_store_b32(tag, rw, WS_GEN, 0, AUX_SC1);
      const float ms = asf(v[0][0]);
      {
        float sv = ms;
        sv += dppf<XOR1>(sv); sv += dppf<XOR2>(sv); sv += dppf<XOR7>(sv);
        const u32 neg = (u32)(__builtin_amdgcn_ballot_w64(sv < 0.f) >> 32), dies = (u32)(__builtin_amdgcn_ballot_w64(v[0][2] & 1) >> 32) & 255u;
        sides = (((neg ^ (dies >> (wg & 7))) & 1) << 8) | (((neg >> 8) ^ dies) & 255u);
        if (lane == 0) *(LAS u32*)(L + L_SD) = sides;
      }
      float Mg = ms;
      Mg = fmaxf(Mg, dppf<XOR1>(Mg)); Mg = fmaxf(Mg, dppf<XOR2>(Mg)); Mg = fmaxf(Mg, dppf<XOR7>(Mg));
      float Lg = asf(v[0][2]) * __builtin_amdgcn_exp2f(ms - Mg);
      Lg += dppf<XOR1>(Lg); Lg += dppf<XOR2>(Lg); Lg += dppf<XOR7>(Lg);
      if (lane < 32) ((LAS float*)(L + L_F))[lane] = __builtin_amdgcn_exp2f(ml[0] - Mg) / Lg;
    }
    __builtin_amdgcn_fence(__ATOMIC_ACQ_REL, "wavefront", "local");
    f32x2 fA[8], fB[8];
#pragma unroll
    for (int i = 0; i < 8; ++i) { fA[i] = *(const LAS f32x2*)(L + L_F + i * 8); fB[i] = *(const LAS f32x2*)(L + L_F + 64 + i * 8); PIN(fB[i]); }
    const v2u oA = merge8(L + L_PART + lane * 16, fA);
    v4u pc0v = {oA[0], tag, 0u, tag}, pc1v = {oA[1], tag, 0u, tag};
    PIN(pc0v); PIN(pc1v);
    gt = *(const LAS f32x4*)(L + L_G); PIN(gt);
    YOFF();
    fo = WS_Y + two_places((fq >> 1) * 8192 + (fq & 1) * 64 + (lane >> 2) * 128 + (lane & 3) * 16, sides >> (wg & 7));
    oo = (lane & 28) == 0 ? ((lane >> 5) * DM + 4 * fq + (lane & 3)) * 2 : 1 << 20;
    PIN(fo); PIN(oo);
    EARLY_READS();
    int o = WS_ACC + two_places((team * 8 + m) * 2048 + lane * 16, sides >> 8);
    PIN(o);
    WOLD(2);
    WOLD(3);
    wg_barrier();
    const v2u oB = merge8(L + L_PART + 8192 + lane * 16, fB);
    pc0v[2] = oB[0]; pc1v[2] = oB[1];
    __builtin_amdgcn_raw_buffer_store_b128(pc0v, rw, o, 0, AUX_SC1);
    __builtin_amdgcn_raw_buffer_store_b128(pc1v, rw, o + 1024, 0, AUX_SC1);
    WOST(2); WOST(3);
  } else {
    const size_t seg = (size_t)(b * HKV + g) * SK * DH;
    const rsrc_t rkB = mkrs(kB + seg, SK * 256), rvB = mkrs(vB + seg, SK * 256);
    const rsrc_t rkA = mkrs(kA + seg + (SK - WA) * DH, WA * 256), rvA = mkrs(vA + seg + (SK - WA) * DH, WA * 256);
    const int qx = PAR(27) | (PAR(11) << 1), sxB = (PAR(40) | (PAR(12) << 1) | (PAR(96) << 2)) << 18, sxA = sxB & (1 << 18);
    int vo[4];
#pragma unroll
    for (int n = 0; n < 4; ++n) vo[n] = lane * 16 + wave * 4096 + m * 32768 + (n ^ qx) * 1024;
    constexpr int NT = 80;
    v4u ld[NT];
#define LIDX(i) ((i) < 32 ? (i) : (i) < 40 ? (i) - 32 : (i) < 48 ? (i) - 40 : (i) - 48)
    int dT = 0;
#define DUE(i) ((OFFK + (i) * TAU + ((i) < 4 ? 0 : (i) < 20 ? (i) - 4 : 16) * RAMP2 + ((i) >= 48) * (GAPA + 2 * TAUO + GAPB) + ((i) < 48 ? 0 : (i) < 56 ? (i) - 48 : 8) * RAMP3 + (((i) >= 32) + ((i) >= 40)) * GAP + ((i) < TAT ? 0 : (i) - TAT + 1 < 16 ? (i) - TAT + 1 : 16) * dT) >> 4)
#define ISSUE(i) do { if ((i) == 48) { WOLD(0); WOLD(1); } if ((i) < NT) { SB(); pace(T0 + DUE(i)); wait_vm<FL - 1>(); SB(); \
      ld[(i) < NT ? (i) : 0] = __builtin_amdgcn_raw_buffer_load_b128((i) < 32 ? rkB : (i) < 40 ? rkA : (i) < 48 ? rvA : rvB, vo[(i) & 3], \
        ((LIDX(i) >> 2) << 18) ^ ((i) >= 32 && (i) < 48 ? sxA : sxB), AUX_NT); SB(); } } while (0)
    xv = *xp;
    SLIP(3);
    if (wave == 7) *(volatile LAS v2u*)(L + L_TS) = v2u{Tb, 1u};
#pragma unroll
    for (int i = 0; i < PRE; ++i) {
      ISSUE(i);
      if (i == QAT - 1) QPART();
      if (i == TAT - 1) {
        wg_barrier();
        const v2u tt = *(const LAS v2u*)(L + L_TC);
        const u32 Tc = __builtin_amdgcn_readfirstlane(tt[0]);
        tag = __builtin_amdgcn_readfirstlane(tt[1]);
        dT = (int)(Tc - Tb);
        SET_TIMES(Tc);
      }
    }
    wg_barrier();
    const int hown = (c >> 1) & 1;
    v4u qa = *(const LAS v4u*)(L + L_QS + 512 + hown * 256 + c * 16), qo = *(const LAS v4u*)(L + L_QS + 512 + (hown ^ 1) * 256 + c * 16);
    float sc[10];
#define IPK(n) ((PRE + ((n) + 1) / 2) > ((n) + FL) ? (PRE + ((n) + 1) / 2) : ((n) + FL))
#define KTILE(t) do { float y[4]; \
      _Pragma("unroll") for (int n = 0; n < 4; ++n) { y[n] = qk_part(ld[4 * (t) + n], qa, qo); if (n == 3) { sc[t] = qk_red(y, c); PIN(sc[t]); } else PIN(y[n]); \
        if (IPK(4 * (t) + n + 1) > IPK(4 * (t) + n)) ISSUE(IPK(4 * (t) + n)); } } while (0)
    KTILE(0); KTILE(1); KTILE(2); KTILE(3); KTILE(4); KTILE(5); KTILE(6); KTILE(7);
    wg_barrier();
    qa = *(const LAS v4u*)(L + L_QS + hown * 256 + c * 16); qo = *(const LAS v4u*)(L + L_QS + (hown ^ 1) * 256 + c * 16);
    float mB = fmaxf(fmaxf(fmaxf(sc[0], sc[1]), fmaxf(sc[2], sc[3])), fmaxf(fmaxf(sc[4], sc[5]), fmaxf(sc[6], sc[7])));
    mB = fmaxf(mB, dppf<ROR4>(mB)); mB = fmaxf(mB, dppf<ROR8>(mB)); mB = xrow_max(mB);
    PIN(mB);
    KTILE(8);
    float lB = 0.f; u32 pp[10];
#pragma unroll
    for (int t = 0; t < 8; ++t) { const float p = __builtin_amdgcn_exp2f(sc[t] - mB); lB += p; pp[t] = cvtpk(p, p); PIN(pp[t]); }
    KTILE(9);
    static_assert(IPK(40) == 40 + FL, "schedule");
    float mA = fmaxf(sc[8], sc[9]);
    mA = fmaxf(mA, dppf<ROR4>(mA)); mA = fmaxf(mA, dppf<ROR8>(mA)); mA = xrow_max(mA);
    const float pA0 = __builtin_amdgcn_exp2f(sc[8] - mA), pA1 = __builtin_amdgcn_exp2f(sc[9] - mA);
    float lA = pA0 + pA1;
    pp[8] = cvtpk(pA0, pA0); pp[9] = cvtpk(pA1, pA1);
    f32x4 acc = {0.f, 0.f, 0.f, 0.f};
#define VSTEP(t, n) do { acc = mfma(pv_frag<n>(pp[(t) < 2 ? 8 + (t) : (t) - 2], msk), ld[40 + 4 * (t) + (n)], acc); PIN(acc); ISSUE(40 + 4 * (t) + (n) + FL); } while (0)
#define VTILE(t) do { VSTEP(t, 0); VSTEP(t, 1); VSTEP(t, 2); VSTEP(t, 3); } while (0)
    VSTEP(0, 0);
    lA += dppf<ROR4>(lA); lA += dppf<ROR8>(lA); lA = xrow_sum(lA);
    lB += dppf<ROR4>(lB); lB += dppf<ROR8>(lB); lB = xrow_sum(lB);
    if ((lane & ~6) == 0) *(LAS f32x2*)(L + L_PML + (lane >> 1) * 64 + wave * 8) = (lane & 4) ? f32x2{mB, lB} : f32x2{mA, lA};
    wg_barrier();
    VSTEP(0, 1); VSTEP(0, 2); VSTEP(0, 3);
    VTILE(1);
    *(LAS f32x4*)(L + L_PART + tid * 16) = acc;
    WOST(0); WOST(1);
    wg_barrier();
    NEW_CODE_PAGE();
    acc = f32x4{0.f, 0.f, 0.f, 0.f};
    VTILE(2); VTILE(3);
    EARLY_READS();
    VTILE(4); VTILE(5); VTILE(6); VTILE(7);
    static_assert(FL >= 4, "every K / V load is issued after tile 8");
    VTILE(8);
    VSTEP(9, 0); VSTEP(9, 1);
    v4u a2 = pv_frag<2>(pp[7], msk), a3 = pv_frag<3>(pp[7], msk);
    int off[1] = {WS_ACC + two_places((((2 * g + (wave >> 2)) * 8 + (lane >> 3)) * 2 + th) * 1024 + (16 * (wave & 3) + 8 * tdh + (lane & 7)) * 16, 0)};
    int oso = L_OS + (lane >> 5) * 256 + (wave >> 2) * 128 + (lane & 7) * 16 + (wave & 3) * 4 + ((lane >> 4) & 1) * 2;
    PIN(a2); PIN(a3); PIN(off[0]); PIN(oso);
    WOLD(2);
    acc = mfma(a3, ld[79], mfma(a2, ld[78], acc));
    *(LAS f32x4*)(L + L_PART + 8192 + tid * 16) = acc;
    WOLD(3);
    wg_barrier();
    gt = *(const LAS f32x4*)(L + L_G); PIN(gt);
    sides = __builtin_amdgcn_readfirstlane(*(const LAS u32*)(L + L_SD));
    off[0] += (sides >> 8) << 13;
    YOFF();
    v4u v[1];
    pace(TE + DA);
    SB();
    v[0] = __builtin_amdgcn_raw_buffer_load_b128(rw, off[0], 0, AUX_SC1);
    SB();
    WOST(2); WOST(3);
    SB();
#define SUM_A() do { auto s0 = __builtin_amdgcn_permlane32_swap(v[0][0] << 16, v[0][2] << 16, false, false); \
      auto s1 = __builtin_amdgcn_permlane32_swap(v[0][0] & 0xffff0000u, v[0][2] & 0xffff0000u, false, false); \
      auto s2 = __builtin_amdgcn_permlane16_swap(asu(asf(s0[0]) + asf(s0[1])), asu(asf(s1[0]) + asf(s1[1])), false, false); \
      float o = asf(s2[0]) + asf(s2[1]); o += dppf<ROR8>(o); *(LAS u16*)(L + oso) = (u16)cvtpk(o, o); } while (0)
    SUM_A();
    SB();
    if (any_stale<1>(v, tag)) { get_pieces<1, 0>(rw, off, tag, v, (v[0][1] == tag) & (v[0][3] == tag)); SUM_A(); }
  }
  {
    wg_barrier();
    const LAS char* pa = L + pao;
    pb = L + pbo;
    const v4u a0 = *(const LAS v4u*)(pa), a1 = *(const LAS v4u*)(pa + 64), a2 = *(const LAS v4u*)(pa + 256), a3 = *(const LAS v4u*)(pa + 320);
    const v4u bf2 = BFRAG(2), bf3 = BFRAG(3);
    SB();
    const f32x4 z = {0.f, 0.f, 0.f, 0.f};
    const f32x4 da = mfma(a1, bf1, mfma(a0, bf0, z)), db = mfma(a3, bf3, mfma(a2, bf2, z));
    put_piece(rw, yo, asu(fmaf(gt[1], db[0], gt[0] * da[0])), asu(fmaf(gt[3], db[1], gt[2] * da[1])), tag);
    SB();
  }
  if (wave == 8 && fq < 144) {
    const int off[4] = {fo, fo + 2048, fo + 4096, fo + 6144};
    const rsrc_t ro = mkrs(out, 2 * DM * 2);
    v4u v[4];
    SB();
    pace(TE + DB);
    SB();
#pragma unroll
    for (int k = 0; k < 4; ++k) v[k] = __builtin_amdgcn_raw_buffer_load_b128(rw, off[k], 0, AUX_SC1);
    SB();
#define SUM_B() do { const float y0 = ((asf(v[0][0]) + asf(v[1][0])) + asf(v[2][0])) + asf(v[3][0]), y1 = ((asf(v[0][2]) + asf(v[1][2])) + asf(v[2][2])) + asf(v[3][2]); \
      auto s0 = __builtin_amdgcn_permlane32_swap(asu(y0), asu(y1), false, false);   \
      float r = asf(s0[0]) + asf(s0[1]); r += dppf<ROR4>(r); r += dppf<ROR8>(r); \
      auto s1 = __builtin_amdgcn_permlane16_swap(asu(r), asu(r), false, false); r = asf(s1[0]) + asf(s1[1]); \
      __builtin_amdgcn_raw_buffer_store_b16((u16)cvtpk(r, r), ro, oo, 0, 0); } while (0)
    SUM_B();
    SB();
    if (any_stale<4>(v, tag)) { get_pieces<4, 0>(rw, off, tag, v); SUM_B(); }
  }
}

extern "C" void kernel_launch(void* const* d_in, const int* in_sizes, int n_in, void* d_out, int out_size, void* d_ws, size_t ws_size, hipStream_t stream) {
  k_fused<<<256, 576, 0, stream>>>((const u16*)d_in[0], (const u16*)d_in[1], (const u16*)d_in[2], (const u16*)d_in[3], (const u16*)d_in[4],
                                   (const u16*)d_in[6], (const u16*)d_in[7], (const u16*)d_in[8], (const u16*)d_in[9], (const u16*)d_in[10],
                                   (u16*)d_out, (char*)d_ws);
}
```
